```python
import torch

def _softmax(x, axis=-1):
    return torch.softmax(x, dim=axis)
def _gelu(x):
    return torch.nn.functional.gelu(x, approximate="tanh")
def _silu(x):
    return torch.nn.functional.silu(x)
def _sigmoid(x):
    return torch.sigmoid(x)
def _layernorm(x, axis=-1, eps=1e-5):
    m = x.mean(dim=axis, keepdim=True)
    v = x.var(dim=axis, keepdim=True, unbiased=False)
    return (x - m) / torch.sqrt(v + eps)
def _scatter_add(tgt, idx, src, axis=0):
    return tgt.clone().index_add_(axis, idx, src)
def _segment_sum(src, seg, n):
    out = torch.zeros((n,) + src.shape[1:], dtype=src.dtype)
    return out.index_add_(0, seg, src)
def _pad(x, pw):
    flat = tuple(v for pair in reversed(pw) for v in pair)
    return torch.nn.functional.pad(x, flat)

kernel_name = 'recurrent_scan__mha_block_cached__geglu_ffn__decode'

def setup_inputs():

    _w = (lambda a: (lambda t: t.to(torch.bfloat16) if t.is_floating_point() else t)(torch.as_tensor(a)))
    def _A_setup():
        B, Sq, D = 2, 1, 512
        H, Sk, Dh = 32, 8192, 128
        return {
            "x": _w((lambda *s: torch.randn(*s, dtype=torch.bfloat16) * 0.05)(B, Sq, D)),
            "k_cache": _w((lambda *s: torch.randn(*s, dtype=torch.bfloat16) * 1.6)(B, H, Sk, Dh)),
            "v_cache": _w((lambda *s: torch.randn(*s, dtype=torch.bfloat16) * 0.2)(B, H, Sk, Dh)),
            "wq": _w((lambda *s: torch.randn(*s, dtype=torch.bfloat16) * 1.6)(D, H * Dh) * (D ** -0.5)),
            "wo": _w((lambda *s: torch.randn(*s, dtype=torch.bfloat16) * 0.2)(H * Dh, D) * ((H * Dh) ** -0.5)),
        }
    _A_in = _A_setup()

    def _B_setup():
        B, S, D, F = 2, 1, 512, 1376
        return {
            "x": _w((lambda *s: torch.randn(*s, dtype=torch.bfloat16) * 0.05)(B, S, D)),
            "w_gate": _w((lambda *s: torch.randn(*s, dtype=torch.bfloat16) * 0.1)(D, F)),
            "w_up": _w((lambda *s: torch.randn(*s, dtype=torch.bfloat16) * 0.1)(D, F)),
            "w_down": _w((lambda *s: torch.randn(*s, dtype=torch.bfloat16) * 0.1)(F, D)),
        }
    _B_in = _B_setup()

    return {
        "A_x": _A_in["x"],
        "A_k_cache": _A_in["k_cache"],
        "A_v_cache": _A_in["v_cache"],
        "A_wq": _A_in["wq"],
        "A_wo": _A_in["wo"],
        "B_x": _B_in["x"],
        "B_w_gate": _B_in["w_gate"],
        "B_w_up": _B_in["w_up"],
        "B_w_down": _B_in["w_down"],
    }

def reference(A_x, A_k_cache, A_v_cache, A_wq, A_wo, B_x, B_w_gate, B_w_up, B_w_down):
    def _A_ref(x, k_cache, v_cache, wq, wo):
        B, Sq, D = x.shape
        H, Dh = 32, 128
        q = (lambda x, *d: x.permute(*d))((x @ wq).reshape(B, Sq, H, Dh), 0, 2, 1, 3)
        scores = torch.einsum("bhqd,bhkd->bhqk", q, k_cache) * (Dh ** -0.5)
        p = _softmax(scores, axis=-1)
        o = torch.einsum("bhqk,bhkd->bhqd", p, v_cache)
        return (lambda x, *d: x.permute(*d))(o, 0, 2, 1, 3).reshape(B, Sq, H * Dh) @ wo

    def _B_ref(x, w_gate, w_up, w_down):
        return (_gelu(x @ w_gate) * (x @ w_up)) @ w_down

    x = A_x
    xn = _layernorm(x, axis=-1)
    s = _A_ref(xn, A_k_cache, A_v_cache, A_wq, A_wo)
    g = _silu(_B_ref(xn, B_w_gate, B_w_up, B_w_down))
    return x + s * g
```

```cpp
#include <hip/hip_runtime.h>
#include <hip/hip_bf16.h>
#include <stdint.h>

typedef unsigned u32x4 __attribute__((ext_vector_type(4)));
typedef float f32x4 __attribute__((ext_vector_type(4)));
typedef __bf16 bf16x2 __attribute__((ext_vector_type(2)));

constexpr int NB = 2, DM = 512, NH = 32, SK = 8192, DH = 128, FF = 1376;
constexpr int NSPLIT = 8;

__device__ __forceinline__ float bf_lo(unsigned u) { return __builtin_bit_cast(float, u << 16); }
__device__ __forceinline__ float bf_hi(unsigned u) { return __builtin_bit_cast(float, u & 0xffff0000u); }
__device__ __forceinline__ float rbf(float f) { return (float)(__bf16)f; }
__device__ __forceinline__ unsigned short f2bfbits(float f) { __bf16 b = (__bf16)f; return __builtin_bit_cast(unsigned short, b); }
__device__ __forceinline__ float dot2(unsigned k, unsigned q, float acc) {
  return __builtin_amdgcn_fdot2_f32_bf16(__builtin_bit_cast(bf16x2, k), __builtin_bit_cast(bf16x2, q), acc, false);
}
__device__ __forceinline__ float wave_sum(float v) {
  for (int o = 32; o > 0; o >>= 1) v += __shfl_xor(v, o);
  return v;
}

__device__ __forceinline__ void layernorm_lds(const unsigned short* x, float* xn) {
  const int tid = threadIdx.x, lane = tid & 63, w = tid >> 6;
  if (w < NB) {
    const u32x4 v = *reinterpret_cast<const u32x4*>(x + w * DM + lane * 8);
    float f[8];
#pragma unroll
    for (int i = 0; i < 4; ++i) { f[2 * i] = bf_lo(v[i]); f[2 * i + 1] = bf_hi(v[i]); }
    float s = 0.f;
#pragma unroll
    for (int i = 0; i < 8; ++i) s += f[i];
    s = wave_sum(s);
    const float mean = s * (1.0f / DM);
    float q = 0.f;
#pragma unroll
    for (int i = 0; i < 8; ++i) { const float d = f[i] - mean; q += d * d; }
    q = wave_sum(q);
    const float var = rbf(q * (1.0f / DM));
    const float mb = rbf(mean);
    const float den = rbf(sqrtf(rbf(var + 1e-5f)));
#pragma unroll
    for (int i = 0; i < 8; ++i) xn[w * DM + lane * 8 + i] = rbf(rbf(f[i] - mb) / den);
  }
  __syncthreads();
}

__device__ __forceinline__ float gelu_tanh(float x) {
  const float k0 = 0.7978845608028654f, k1 = 0.044715f;
  const float inner = k0 * (x + k1 * x * x * x);
  return 0.5f * x * (1.0f + tanhf(inner));
}

__global__ void __launch_bounds__(256) k_prep(const unsigned short* x, const unsigned short* wq, const unsigned short* wg,
                                              const unsigned short* wu, unsigned short* qout, float* hout) {
  __shared__ float xn[NB * DM];
  __shared__ float red[32][2][64 + 1];
  __shared__ float red2[2][64];
  layernorm_lds(x, xn);
  const int tid = threadIdx.x;
  const int cg = tid & 7, rg = tid >> 3;
  float acc[2][8];
#pragma unroll
  for (int b = 0; b < 2; ++b)
#pragma unroll
    for (int i = 0; i < 8; ++i) acc[b][i] = 0.f;
  const unsigned short* base; int ld;
  if (blockIdx.x < 64) { base = wq + blockIdx.x * 64 + cg * 8; ld = NH * DH; }
  else { const int fb = blockIdx.x - 64; base = (cg < 4 ? wg : wu) + fb * 32 + (cg & 3) * 8; ld = FF; }
  u32x4 v[16];
#pragma unroll
  for (int i = 0; i < 16; ++i) v[i] = *reinterpret_cast<const u32x4*>(base + (size_t)(rg + 32 * i) * ld);
#pragma unroll
  for (int i = 0; i < 16; ++i) {
    const float x0 = xn[rg + 32 * i], x1 = xn[DM + rg + 32 * i];
#pragma unroll
    for (int j = 0; j < 4; ++j) {
      acc[0][2 * j] += x0 * bf_lo(v[i][j]); acc[0][2 * j + 1] += x0 * bf_hi(v[i][j]);
      acc[1][2 * j] += x1 * bf_lo(v[i][j]); acc[1][2 * j + 1] += x1 * bf_hi(v[i][j]);
    }
  }
#pragma unroll
  for (int b = 0; b < 2; ++b)
#pragma unroll
    for (int i = 0; i < 8; ++i) red[rg][b][cg * 8 + i] = acc[b][i];
  __syncthreads();
  if (tid < 128) {
    const int b = tid >> 6, c = tid & 63;
    float s = 0.f;
    for (int r = 0; r < 32; ++r) s += red[r][b][c];
    if (blockIdx.x < 64) qout[b * (NH * DH) + blockIdx.x * 64 + c] = f2bfbits(s);
    else red2[b][c] = rbf(s);
  }
  __syncthreads();
  if (blockIdx.x >= 64 && tid < 64) {
    const int b = tid >> 5, c = tid & 31, fb = blockIdx.x - 64;
    const float g = rbf(gelu_tanh(red2[b][c]));
    hout[b * FF + fb * 32 + c] = rbf(g * red2[b][32 + c]);
  }
}

template <int CTRL> __device__ __forceinline__ float dpp(float x) {
  return __builtin_bit_cast(float, __builtin_amdgcn_mov_dpp(__builtin_bit_cast(int, x), CTRL, 0xf, 0xf, true));
}

__global__ void __launch_bounds__(256) k_attn(const unsigned short* q, const unsigned short* K, const unsigned short* V, float* po, float* pml) {
  __shared__ float mrg[4][4 + DH];
  const int bh = blockIdx.x / NSPLIT, sp = blockIdx.x % NSPLIT;
  const int tid = threadIdx.x, lane = tid & 63, w = __builtin_amdgcn_readfirstlane(tid >> 6), r = lane >> 4, c = lane & 15;
  constexpr int KPS = SK / NSPLIT;
  const u32x4 qv = *reinterpret_cast<const u32x4*>(q + bh * DH + c * 8);
  const size_t base = ((size_t)bh * SK + sp * KPS) * DH;
  const u32x4* kp = reinterpret_cast<const u32x4*>(K + base) + lane;
  const u32x4* vp = reinterpret_cast<const u32x4*>(V + base) + lane;
  float m = -INFINITY, l = 0.f, acc[8];
#pragma unroll
  for (int i = 0; i < 8; ++i) acc[i] = 0.f;
  constexpr int U = 8;
  constexpr int NSTEP = KPS / (4 * U * 4);
  for (int s = 0; s < NSTEP; ++s) {
    u32x4 kk[U], vv[U];
    const int off = (s * 4 + w) * U * 64;
#pragma unroll
    for (int u = 0; u < U; ++u) kk[u] = __builtin_nontemporal_load(kp + off + u * 64);
#pragma unroll
    for (int u = 0; u < U; ++u) vv[u] = __builtin_nontemporal_load(vp + off + u * 64);
    float sc[U];
#pragma unroll
    for (int u = 0; u < U; ++u) {
      float d = 0.f;
#pragma unroll
      for (int j = 0; j < 4; ++j) d = dot2(kk[u][j], qv[j], d);
      d += dpp<0xB1>(d); d += dpp<0x4E>(d); d += dpp<0x141>(d); d += dpp<0x128>(d);
      sc[u] = rbf(rbf(d) * 0.08838834764831845f) * 1.4426950408889634f;
    }
    float mx = m;
#pragma unroll
    for (int u = 0; u < U; ++u) mx = fmaxf(mx, sc[u]);
    const float al = __builtin_amdgcn_exp2f(m - mx);
    m = mx; l *= al;
#pragma unroll
    for (int i = 0; i < 8; ++i) acc[i] *= al;
#pragma unroll
    for (int u = 0; u < U; ++u) {
      const float p = __builtin_amdgcn_exp2f(sc[u] - mx);
      l += p;
#pragma unroll
      for (int j = 0; j < 4; ++j) { acc[2 * j] += p * bf_lo(vv[u][j]); acc[2 * j + 1] += p * bf_hi(vv[u][j]); }
    }
  }
  float M = fmaxf(m, __shfl_xor(m, 16)); M = fmaxf(M, __shfl_xor(M, 32));
  const float sc = __builtin_amdgcn_exp2f(m - M);
  l *= sc; l += __shfl_xor(l, 16); l += __shfl_xor(l, 32);
#pragma unroll
  for (int i = 0; i < 8; ++i) { float a = acc[i] * sc; a += __shfl_xor(a, 16); a += __shfl_xor(a, 32); acc[i] = a; }
  if (r == 0) {
#pragma unroll
    for (int i = 0; i < 8; ++i) mrg[w][4 + c * 8 + i] = acc[i];
    if (c == 0) { mrg[w][0] = M; mrg[w][1] = l; }
  }
  __syncthreads();
  if (tid < DH) {
    float MM = fmaxf(fmaxf(mrg[0][0], mrg[1][0]), fmaxf(mrg[2][0], mrg[3][0]));
    float L = 0.f, o = 0.f;
#pragma unroll
    for (int i = 0; i < 4; ++i) { const float wt = __builtin_amdgcn_exp2f(mrg[i][0] - MM); L += wt * mrg[i][1]; o += wt * mrg[i][4 + tid]; }
    po[(size_t)blockIdx.x * DH + tid] = o;
    if (tid == 0) { pml[blockIdx.x * 2] = MM; pml[blockIdx.x * 2 + 1] = L; }
  }
}

__global__ void __launch_bounds__(256) k_final(const unsigned short* x, const float* po, const float* pml, const float* h,
                                               const unsigned short* wo, const unsigned short* wd, unsigned short* out) {
  __shared__ float o[NB * NH * DH];
  __shared__ float red[4][2][2][8];
  const int tid = threadIdx.x;
  for (int i = tid; i < NB * NH * DH; i += 256) {
    const int bh = i / DH, d = i % DH;
    float M = -INFINITY;
#pragma unroll
    for (int s = 0; s < NSPLIT; ++s) M = fmaxf(M, pml[(bh * NSPLIT + s) * 2]);
    float L = 0.f, a = 0.f;
#pragma unroll
    for (int s = 0; s < NSPLIT; ++s) { const float wt = __builtin_amdgcn_exp2f(pml[(bh * NSPLIT + s) * 2] - M); L += wt * pml[(bh * NSPLIT + s) * 2 + 1]; a += wt * po[(size_t)(bh * NSPLIT + s) * DH + d]; }
    o[i] = rbf(a / L);
  }
  __syncthreads();
  float as[2][8], ay[2][8];
#pragma unroll
  for (int b = 0; b < 2; ++b)
#pragma unroll
    for (int i = 0; i < 8; ++i) { as[b][i] = 0.f; ay[b][i] = 0.f; }
  const int c0 = blockIdx.x * 8;
  for (int r = tid; r < NH * DH; r += 256) {
    const u32x4 v = *reinterpret_cast<const u32x4*>(wo + (size_t)r * DM + c0);
    const float x0 = o[r], x1 = o[NH * DH + r];
#pragma unroll
    for (int j = 0; j < 4; ++j) {
      as[0][2 * j] += x0 * bf_lo(v[j]); as[0][2 * j + 1] += x0 * bf_hi(v[j]);
      as[1][2 * j] += x1 * bf_lo(v[j]); as[1][2 * j + 1] += x1 * bf_hi(v[j]);
    }
  }
  for (int r = tid; r < FF; r += 256) {
    const u32x4 v = *reinterpret_cast<const u32x4*>(wd + (size_t)r * DM + c0);
    const float x0 = h[r], x1 = h[FF + r];
#pragma unroll
    for (int j = 0; j < 4; ++j) {
      ay[0][2 * j] += x0 * bf_lo(v[j]); ay[0][2 * j + 1] += x0 * bf_hi(v[j]);
      ay[1][2 * j] += x1 * bf_lo(v[j]); ay[1][2 * j + 1] += x1 * bf_hi(v[j]);
    }
  }
  const int lane = tid & 63, w = tid >> 6;
#pragma unroll
  for (int b = 0; b < 2; ++b)
#pragma unroll
    for (int i = 0; i < 8; ++i) {
      const float s1 = wave_sum(as[b][i]), s2 = wave_sum(ay[b][i]);
      if (lane == 0) { red[w][0][b][i] = s1; red[w][1][b][i] = s2; }
    }
  __syncthreads();
  if (tid < 16) {
    const int b = tid >> 3, i = tid & 7;
    const float s = rbf(red[0][0][b][i] + red[1][0][b][i] + red[2][0][b][i] + red[3][0][b][i]);
    const float y = rbf(red[0][1][b][i] + red[1][1][b][i] + red[2][1][b][i] + red[3][1][b][i]);
    const float g = rbf(y / (1.0f + __expf(-y)));
    const float xv = bf_lo(x[b * DM + c0 + i]);
    out[b * DM + c0 + i] = f2bfbits(xv + rbf(s * g));
  }
}

extern "C" void kernel_launch(void* const* d_in, const int* in_sizes, int n_in, void* d_out, int out_size, void* d_ws, size_t ws_size,
                              hipStream_t stream) {
  const unsigned short* x = (const unsigned short*)d_in[0];
  const unsigned short* K = (const unsigned short*)d_in[1];
  const unsigned short* V = (const unsigned short*)d_in[2];
  const unsigned short* wq = (const unsigned short*)d_in[3];
  const unsigned short* wo = (const unsigned short*)d_in[4];
  const unsigned short* wg = (const unsigned short*)d_in[6];
  const unsigned short* wu = (const unsigned short*)d_in[7];
  const unsigned short* wd = (const unsigned short*)d_in[8];
  char* ws = (char*)d_ws;
  unsigned short* q = (unsigned short*)ws;
  float* h = (float*)(ws + 65536);
  float* pml = (float*)(ws + 131072);
  float* po = (float*)(ws + 262144);
  k_prep<<<64 + 43, 256, 0, stream>>>(x, wq, wg, wu, q, h);
  k_attn<<<64 * NSPLIT, 256, 0, stream>>>(q, K, V, po, pml);
  k_final<<<64, 256, 0, stream>>>(x, po, pml, h, wo, wd, (unsigned short*)d_out);
}
```

```cpp
#include <hip/hip_runtime.h>
#include <hip/hip_bf16.h>
#include <stdint.h>

typedef unsigned u32x4 __attribute__((ext_vector_type(4)));
typedef float f32x4 __attribute__((ext_vector_type(4)));
typedef __bf16 bf16x2 __attribute__((ext_vector_type(2)));
typedef unsigned short u16;
typedef unsigned long long u64;
#define GAS __attribute__((address_space(1)))
#define LAS __attribute__((address_space(3)))
#define RLX_AGENT __ATOMIC_RELAXED, __HIP_MEMORY_SCOPE_AGENT

#define WO_AUX 2
#define WO_ROWS DH
#define WO_INFL 8
constexpr int DM = 512, NH = 32, SK = 8192, DH = 128, FF = 1376;
constexpr int NWG = 256, NSW = 8, NT = (NSW + 2) * 64;
constexpr int NSPLIT = 4, KPS = SK / NSPLIT, NSTEP = KPS / (16 * NSW);
constexpr int NFFN = FF / 32;
constexpr size_t WS_E = 0, WS_ML = 4096, WS_QP = 16384, WS_GU = WS_QP + (size_t)NH * 8 * 2 * DH * 8, WS_Y = WS_GU + (size_t)48 * 4 * 128 * 8,
                 WS_Z = WS_Y + (size_t)NWG * DM * 8, WS_END = WS_Z + (size_t)2 * 48 * DM * 8;
#define STAMP(i) do {} while (0)
constexpr int L_WO = 0, L_XQ = 131072, L_Q = L_XQ + 512, L_MG = L_Q + 256, L_YR = L_MG + NSW * 132 * 4, L_TAG = L_YR + NSW * DM * 4, L_END = L_TAG + 16;
constexpr unsigned SPIN_CAP = 1u << 20;

struct Params {
  const u16 *x, *K, *V, *wq, *wo, *wg, *wu, *wd;
  u16* out;
  unsigned char* ws;
};

__device__ __forceinline__ float bf_lo(unsigned u) { return __builtin_bit_cast(float, u << 16); }
__device__ __forceinline__ float bf_hi(unsigned u) { return __builtin_bit_cast(float, u & 0xffff0000u); }
__device__ __forceinline__ float rbf(float f) { return (float)(__bf16)f; }
__device__ __forceinline__ unsigned bfbits(float f) { __bf16 b = (__bf16)f; return (unsigned)__builtin_bit_cast(u16, b); }
__device__ __forceinline__ float dot2(unsigned k, unsigned q, float acc) {
  return __builtin_amdgcn_fdot2_f32_bf16(__builtin_bit_cast(bf16x2, k), __builtin_bit_cast(bf16x2, q), acc, false);
}
template <int CTRL> __device__ __forceinline__ float dpp(float x) {
  return __builtin_bit_cast(float, __builtin_amdgcn_mov_dpp(__builtin_bit_cast(int, x), CTRL, 0xf, 0xf, true));
}
constexpr int XOR1 = 0xB1, XOR2 = 0x4E, XOR7 = 0x141, ROR8 = 0x128, ROR4 = 0x124;
__device__ __forceinline__ float xrow_sum(float x) {
  auto s = __builtin_amdgcn_permlane16_swap(__float_as_uint(x), __float_as_uint(x), false, false);
  x = __uint_as_float(s[0]) + __uint_as_float(s[1]);
  auto t = __builtin_amdgcn_permlane32_swap(__float_as_uint(x), __float_as_uint(x), false, false);
  return __uint_as_float(t[0]) + __uint_as_float(t[1]);
}
__device__ __forceinline__ float xrow_max(float x) {
  auto s = __builtin_amdgcn_permlane16_swap(__float_as_uint(x), __float_as_uint(x), false, false);
  x = fmaxf(__uint_as_float(s[0]), __uint_as_float(s[1]));
  auto t = __builtin_amdgcn_permlane32_swap(__float_as_uint(x), __float_as_uint(x), false, false);
  return fmaxf(__uint_as_float(t[0]), __uint_as_float(t[1]));
}
__device__ __forceinline__ float row_sum(float d) {
  d += dpp<XOR1>(d); d += dpp<XOR2>(d); d += dpp<XOR7>(d); d += dpp<ROR8>(d);
  return d;
}
__device__ __forceinline__ float wave_sum(float d) { return xrow_sum(row_sum(d)); }
__device__ __forceinline__ u32x4 ldnt(const GAS u32x4* p) { return __builtin_nontemporal_load(p); }
__device__ __forceinline__ void st_gran(GAS u64* g, unsigned tag, float v) {
  __hip_atomic_store(g, ((u64)tag << 32) | (u64)__float_as_uint(v), RLX_AGENT);
}
__device__ __forceinline__ float gval(u64 g) { return __uint_as_float((unsigned)g); }

__device__ __forceinline__ void ln_stats(u32x4 v, float& mb, float& den) {
  float f[8];
#pragma unroll
  for (int i = 0; i < 4; ++i) { f[2 * i] = bf_lo(v[i]); f[2 * i + 1] = bf_hi(v[i]); }
  float s = ((f[0] + f[1]) + (f[2] + f[3])) + ((f[4] + f[5]) + (f[6] + f[7]));
  s = wave_sum(s);
  const float mean = s * (1.0f / DM);
  float q = 0.f;
#pragma unroll
  for (int i = 0; i < 8; ++i) { const float d = f[i] - mean; q = fmaf(d, d, q); }
  q = wave_sum(q);
  const float var = rbf(q * (1.0f / DM));
  mb = rbf(mean);
  den = rbf(sqrtf(rbf(var + 1e-5f)));
}
__device__ __forceinline__ float ln_apply(float x, float mb, float den) { return rbf(rbf(x - mb) / den); }

__device__ __forceinline__ float gelu_tanh(float x) {
  const float inner = 0.7978845608028654f * (x + 0.044715f * x * x * x);
  return 0.5f * x * (1.0f + tanhf(inner));
}

__global__ void __launch_bounds__(NT) fused_decode(Params p) {
  __shared__ __attribute__((aligned(16))) unsigned char smem[L_END];
  LAS unsigned char* L = (LAS unsigned char*)smem;
  const int tid = threadIdx.x, lane = tid & 63, w = __builtin_amdgcn_readfirstlane(tid >> 6);
  const int bid = blockIdx.x, xg = bid & 7, rk = bid >> 3;
  const int h = xg * 4 + (rk >> 3), b = (rk >> 2) & 1, sp = rk & 3;
  const int prod = b * 128 + h * 4 + sp;
  const int r = lane >> 4, c = lane & 15;
  STAMP(0);
  if (w == NSW) {
    __builtin_amdgcn_s_setprio(3);
    const int i8 = rk & 7;
    const unsigned e0 = __hip_atomic_load((GAS unsigned*)(p.ws + WS_E), RLX_AGENT);
    const u32x4 x0 = *(const GAS u32x4*)(p.x + lane * 8), x1 = *(const GAS u32x4*)(p.x + DM + lane * 8);
    const unsigned xa = p.x[64 * i8 + lane], xb = p.x[DM + 64 * i8 + lane];
    u32x4 wqv[16];
    {
      const GAS u16* src = (const GAS u16*)p.wq + (size_t)(64 * i8 + 16 * r) * (NH * DH) + h * DH + c * 8;
#pragma unroll
      for (int i = 0; i < 16; ++i) wqv[i] = ldnt((const GAS u32x4*)(src + (size_t)i * (NH * DH)));
    }
    __builtin_amdgcn_sched_barrier(0);
    const unsigned tag = e0 + 1u;
    float m0, d0, m1, d1;
    ln_stats(x0, m0, d0); ln_stats(x1, m1, d1);
    LAS float* xq = (LAS float*)(L + L_XQ);
    xq[lane] = ln_apply(bf_lo(xa), m0, d0);
    xq[64 + lane] = ln_apply(bf_lo(xb), m1, d1);
    if (lane == 0) *(volatile LAS unsigned*)(L + L_TAG) = tag;
    f32x4 xr0[4], xr1[4];
#pragma unroll
    for (int i = 0; i < 4; ++i) { xr0[i] = *(LAS f32x4*)(xq + 16 * r + 4 * i); xr1[i] = *(LAS f32x4*)(xq + 64 + 16 * r + 4 * i); }
    float a0[8], a1[8];
#pragma unroll
    for (int i = 0; i < 8; ++i) a0[i] = a1[i] = 0.f;
#pragma unroll
    for (int i = 0; i < 16; ++i) {
      const float s0 = xr0[i >> 2][i & 3], s1 = xr1[i >> 2][i & 3];
#pragma unroll
      for (int k = 0; k < 4; ++k) {
        const float lo = bf_lo(wqv[i][k]), hi = bf_hi(wqv[i][k]);
        a0[2 * k] = fmaf(s0, lo, a0[2 * k]); a0[2 * k + 1] = fmaf(s0, hi, a0[2 * k + 1]);
        a1[2 * k] = fmaf(s1, lo, a1[2 * k]); a1[2 * k + 1] = fmaf(s1, hi, a1[2 * k + 1]);
      }
    }
#pragma unroll
    for (int i = 0; i < 8; ++i) { a0[i] = xrow_sum(a0[i]); a1[i] = xrow_sum(a1[i]); }
    {
      GAS u64* qp = (GAS u64*)(p.ws + WS_QP) + (size_t)((h * 8 + i8) * 2 + (r >> 1)) * DH + c * 8 + (r & 1) * 4;
#pragma unroll
      for (int k = 0; k < 4; ++k) {
        const float t0 = a0[k], t1 = a0[4 + k], t2 = a1[k], t3 = a1[4 + k];
        const float lo = (r & 1) ? t1 : t0, hi = (r & 1) ? t3 : t2;
        st_gran(qp + k, tag, (r & 2) ? hi : lo);
      }
    }
    STAMP(1);
    {
      const GAS u64* qg = (const GAS u64*)(p.ws + WS_QP) + (size_t)(h * 8 * 2 + b) * DH + lane;
      u64 g[16];
      for (unsigned spins = 0; spins < SPIN_CAP; ++spins) {
#pragma unroll
        for (int i = 0; i < 8; ++i) { g[2 * i] = __hip_atomic_load(qg + i * 2 * DH, RLX_AGENT); g[2 * i + 1] = __hip_atomic_load(qg + i * 2 * DH + 64, RLX_AGENT); }
        bool ok = true;
#pragma unroll
        for (int i = 0; i < 16; ++i) ok &= (unsigned)(g[i] >> 32) == tag;
        if (__all(ok)) break;
      }
      float q0 = 0.f, q1 = 0.f;
#pragma unroll
      for (int i = 0; i < 8; ++i) { q0 += gval(g[2 * i]); q1 += gval(g[2 * i + 1]); }
      LAS u16* ql = (LAS u16*)(L + L_Q);
      ql[lane] = (u16)bfbits(q0); ql[64 + lane] = (u16)bfbits(q1);
    }
    STAMP(2);
    __syncthreads();
    {
      const GAS u16* src = (const GAS u16*)p.wo + (size_t)h * DH * DM + lane * 8;
#pragma unroll 1
      for (int i0 = 0; i0 < WO_ROWS; i0 += 4) {
#pragma unroll
        for (int i = i0; i < i0 + 4; ++i)
          __builtin_amdgcn_global_load_lds((const GAS unsigned*)(src + i * DM), (LAS unsigned*)(L + L_WO + i * 1024), 16, 0, WO_AUX);
        asm volatile("s_waitcnt vmcnt(%0)" :: "i"(WO_INFL) : "memory");
      }
    }
    asm volatile("s_waitcnt vmcnt(0)" ::: "memory");
    STAMP(3);
    return;
  }
  if (w == NSW + 1) {
    __builtin_amdgcn_s_setprio(2);
    __syncthreads();
    const unsigned tag = *(volatile LAS unsigned*)(L + L_TAG);
    LAS float* sc = (LAS float*)(L + L_YR);
    if (rk < 22) {
      const int j = (xg & 1) * 22 + rk, rq = xg >> 1;
      if (j < NFFN) {
        const int rg = lane >> 2, c4 = lane & 3;
        const u32x4 x0 = *(const GAS u32x4*)(p.x + lane * 8), x1 = *(const GAS u32x4*)(p.x + DM + lane * 8);
        const unsigned xa = *(const GAS unsigned*)(p.x + 128 * rq + 2 * lane), xb = *(const GAS unsigned*)(p.x + DM + 128 * rq + 2 * lane);
        const GAS u16* gp = (const GAS u16*)p.wg + (size_t)(128 * rq + rg) * FF + j * 32 + c4 * 8;
        const GAS u16* up = (const GAS u16*)p.wu + (size_t)(128 * rq + rg) * FF + j * 32 + c4 * 8;
        u32x4 g[8], u[8];
#pragma unroll
        for (int i = 0; i < 8; ++i) { g[i] = ldnt((const GAS u32x4*)(gp + (size_t)i * 16 * FF)); u[i] = ldnt((const GAS u32x4*)(up + (size_t)i * 16 * FF)); }
        float m0, d0, m1, d1;
        ln_stats(x0, m0, d0); ln_stats(x1, m1, d1);
        sc[2 * lane] = ln_apply(bf_lo(xa), m0, d0); sc[2 * lane + 1] = ln_apply(bf_hi(xa), m0, d0);
        sc[128 + 2 * lane] = ln_apply(bf_lo(xb), m1, d1); sc[128 + 2 * lane + 1] = ln_apply(bf_hi(xb), m1, d1);
        float ag[2][8], au[2][8];
#pragma unroll
        for (int i = 0; i < 8; ++i) { ag[0][i] = ag[1][i] = au[0][i] = au[1][i] = 0.f; }
#pragma unroll
        for (int i = 0; i < 8; ++i) {
          const float a0 = sc[i * 16 + rg], a1 = sc[128 + i * 16 + rg];
#pragma unroll
          for (int k = 0; k < 4; ++k) {
            const float gl = bf_lo(g[i][k]), gh = bf_hi(g[i][k]), ul = bf_lo(u[i][k]), uh = bf_hi(u[i][k]);
            ag[0][2 * k] = fmaf(a0, gl, ag[0][2 * k]); ag[0][2 * k + 1] = fmaf(a0, gh, ag[0][2 * k + 1]);
            ag[1][2 * k] = fmaf(a1, gl, ag[1][2 * k]); ag[1][2 * k + 1] = fmaf(a1, gh, ag[1][2 * k + 1]);
            au[0][2 * k] = fmaf(a0, ul, au[0][2 * k]); au[0][2 * k + 1] = fmaf(a0, uh, au[0][2 * k + 1]);
            au[1][2 * k] = fmaf(a1, ul, au[1][2 * k]); au[1][2 * k + 1] = fmaf(a1, uh, au[1][2 * k + 1]);
          }
        }
#pragma unroll
        for (int bb = 0; bb < 2; ++bb)
#pragma unroll
          for (int i = 0; i < 8; ++i) {
            float gs = ag[bb][i], us = au[bb][i];
            gs += dpp<ROR4>(gs); gs += dpp<ROR8>(gs); gs = xrow_sum(gs);
            us += dpp<ROR4>(us); us += dpp<ROR8>(us); us = xrow_sum(us);
            if (rg == 0) { sc[256 + bb * 32 + c4 * 8 + i] = gs; sc[320 + bb * 32 + c4 * 8 + i] = us; }
          }
        GAS u64* gu = (GAS u64*)(p.ws + WS_GU) + (size_t)(j * 4 + rq) * 128 + lane;
        st_gran(gu, tag, sc[256 + lane]); st_gran(gu + 64, tag, sc[320 + lane]);
      }
    } else if (rk < 28) {
      const int j = xg * 6 + (rk - 22);
      if (j < NFFN) {
        const GAS u64* gu = (const GAS u64*)(p.ws + WS_GU) + (size_t)j * 4 * 128 + lane;
        u64 g[8];
        for (unsigned spins = 0; spins < SPIN_CAP; ++spins) {
          __builtin_amdgcn_s_sleep(32);
#pragma unroll
          for (int i = 0; i < 8; ++i) g[i] = __hip_atomic_load(gu + i * 64, RLX_AGENT);
          bool ok = true;
#pragma unroll
          for (int i = 0; i < 8; ++i) ok &= (unsigned)(g[i] >> 32) == tag;
          if (__all(ok)) break;
        }
        const float gs = rbf((gval(g[0]) + gval(g[2])) + (gval(g[4]) + gval(g[6]))), us = rbf((gval(g[1]) + gval(g[3])) + (gval(g[5]) + gval(g[7])));
        sc[lane] = rbf(rbf(gelu_tanh(gs)) * us);
        float z[2][8];
#pragma unroll
        for (int i = 0; i < 8; ++i) z[0][i] = z[1][i] = 0.f;
        const GAS u16* dp = (const GAS u16*)p.wd + (size_t)j * 32 * DM + lane * 8;
#pragma unroll 1
        for (int it = 0; it < 4; ++it) {
          u32x4 d[8];
#pragma unroll
          for (int i = 0; i < 8; ++i) d[i] = ldnt((const GAS u32x4*)(dp + (it * 8 + i) * DM));
#pragma unroll
          for (int i = 0; i < 8; ++i) {
            const float a0 = sc[it * 8 + i], a1 = sc[32 + it * 8 + i];
#pragma unroll
            for (int k = 0; k < 4; ++k) {
              const float lo = bf_lo(d[i][k]), hi = bf_hi(d[i][k]);
              z[0][2 * k] = fmaf(a0, lo, z[0][2 * k]); z[0][2 * k + 1] = fmaf(a0, hi, z[0][2 * k + 1]);
              z[1][2 * k] = fmaf(a1, lo, z[1][2 * k]); z[1][2 * k + 1] = fmaf(a1, hi, z[1][2 * k + 1]);
            }
          }
        }
#pragma unroll
        for (int bb = 0; bb < 2; ++bb) {
          *(LAS f32x4*)(sc + 64 + bb * DM + lane * 8) = f32x4{z[bb][0], z[bb][1], z[bb][2], z[bb][3]};
          *(LAS f32x4*)(sc + 64 + bb * DM + lane * 8 + 4) = f32x4{z[bb][4], z[bb][5], z[bb][6], z[bb][7]};
        }
        GAS u64* zg = (GAS u64*)(p.ws + WS_Z);
#pragma unroll
        for (int bb = 0; bb < 2; ++bb)
#pragma unroll
          for (int i = 0; i < 8; ++i) st_gran(zg + (size_t)(bb * 48 + j) * DM + i * 64 + lane, tag, sc[64 + bb * DM + i * 64 + lane]);
      }
    }
    STAMP(4);
    return;
  }
  const size_t kvbase = ((size_t)(b * NH + h) * SK + (size_t)sp * KPS) * DH / 8 + (size_t)w * 256 + lane;
  const GAS u32x4* kp = (const GAS u32x4*)p.K + kvbase;
  const GAS u32x4* vp = (const GAS u32x4*)p.V + kvbase;
  u32x4 kk[2][4], vv[2][4];
#pragma unroll
  for (int d = 0; d < 2; ++d) {
#pragma unroll
    for (int u = 0; u < 4; ++u) kk[d][u] = *(kp + d * (NSW * 256) + u * 64);
#pragma unroll
    for (int u = 0; u < 4; ++u) vv[d][u] = ldnt(vp + d * (NSW * 256) + u * 64);
  }
  __builtin_amdgcn_sched_barrier(0);
  STAMP(6);
  __syncthreads();
  const u32x4 qv = *(LAS u32x4*)(L + L_Q + c * 16);
  const unsigned tag = *(volatile LAS unsigned*)(L + L_TAG);
  STAMP(1);
  float m = -INFINITY, l = 0.f, acc[8];
#pragma unroll
  for (int i = 0; i < 8; ++i) acc[i] = 0.f;
  constexpr float SCALE = 0.08838834764831845f, LOG2E = 1.4426950408889634f;
#define STEP(D, REFILL, OFF)                                                                      \
  {                                                                                               \
    float sc[4];                                                                                  \
    _Pragma("unroll") for (int u = 0; u < 4; ++u) {                                               \
      float d = dot2(kk[D][u][0], qv[0], 0.f);                                                    \
      d = dot2(kk[D][u][1], qv[1], d); d = dot2(kk[D][u][2], qv[2], d); d = dot2(kk[D][u][3], qv[3], d); \
      sc[u] = d;                                                                                  \
    }                                                                                             \
    __builtin_amdgcn_sched_barrier(0);                                                            \
    if (REFILL) { _Pragma("unroll") for (int u = 0; u < 4; ++u) kk[D][u] = *(kp + (OFF) + u * 64); } \
    __builtin_amdgcn_sched_barrier(0);                                                            \
    _Pragma("unroll") for (int u = 0; u < 4; ++u) sc[u] = rbf(rbf(row_sum(sc[u])) * SCALE) * LOG2E; \
    const float mx = fmaxf(fmaxf(m, fmaxf(sc[0], sc[1])), fmaxf(sc[2], sc[3]));                   \
    const float al = __builtin_amdgcn_exp2f(m - mx);                                              \
    m = mx;                                                                                       \
    float pp[4];                                                                                  \
    _Pragma("unroll") for (int u = 0; u < 4; ++u) pp[u] = __builtin_amdgcn_exp2f(sc[u] - mx);     \
    l = fmaf(l, al, (pp[0] + pp[1]) + (pp[2] + pp[3]));                                           \
    _Pragma("unroll") for (int i = 0; i < 8; ++i) acc[i] *= al;                                   \
    _Pragma("unroll") for (int u = 0; u < 4; ++u)                                                 \
      _Pragma("unroll") for (int k = 0; k < 4; ++k) {                                             \
        acc[2 * k] = fmaf(pp[u], bf_lo(vv[D][u][k]), acc[2 * k]);                                 \
        acc[2 * k + 1] = fmaf(pp[u], bf_hi(vv[D][u][k]), acc[2 * k + 1]);                         \
      }                                                                                           \
    __builtin_amdgcn_sched_barrier(0);                                                            \
    if (REFILL) { _Pragma("unroll") for (int u = 0; u < 4; ++u) vv[D][u] = ldnt(vp + (OFF) + u * 64); } \
    __builtin_amdgcn_sched_barrier(0);                                                            \
  }
#pragma unroll 1
  for (int s = 2; s < NSTEP; s += 2) {
    STEP(0, true, s * (NSW * 256))
    STEP(1, true, (s + 1) * (NSW * 256))
  }
  STEP(0, false, 0)
  STEP(1, false, 0)
#undef STEP
  STAMP(2);
  {
    const float M = xrow_max(m);
    const float f = __builtin_amdgcn_exp2f(m - M);
    l = xrow_sum(l * f);
#pragma unroll
    for (int i = 0; i < 8; ++i) acc[i] = xrow_sum(acc[i] * f);
    LAS float* mg = (LAS float*)(L + L_MG) + w * 132;
    if (r == 0) {
      *(LAS f32x4*)(mg + 4 + c * 8) = f32x4{acc[0], acc[1], acc[2], acc[3]};
      *(LAS f32x4*)(mg + 8 + c * 8) = f32x4{acc[4], acc[5], acc[6], acc[7]};
      if (c == 0) { mg[0] = M; mg[1] = l; }
    }
  }
  __syncthreads();
  STAMP(7);
  {
    LAS float* mg = (LAS float*)(L + L_MG);
    float mw[NSW], M = -INFINITY;
#pragma unroll
    for (int i = 0; i < NSW; ++i) { mw[i] = mg[i * 132]; M = fmaxf(M, mw[i]); }
    float Ls = 0.f;
    f32x4 o[4];
#pragma unroll
    for (int k = 0; k < 4; ++k) o[k] = f32x4{0.f, 0.f, 0.f, 0.f};
#pragma unroll
    for (int i = 0; i < NSW; ++i) {
      const float f = __builtin_amdgcn_exp2f(mw[i] - M);
      Ls = fmaf(f, mg[i * 132 + 1], Ls);
#pragma unroll
      for (int k = 0; k < 4; ++k) o[k] += f * *(LAS f32x4*)(mg + i * 132 + 4 + 16 * w + 4 * k);
    }
    if (tid == 0) {
      GAS u64* ml = (GAS u64*)(p.ws + WS_ML) + prod * 2;
      st_gran(ml, tag, M); st_gran(ml + 1, tag, Ls);
    }
    float y[8];
#pragma unroll
    for (int i = 0; i < 8; ++i) y[i] = 0.f;
#pragma unroll
    for (int i = 0; i < 16; ++i) {
      const u32x4 wv = *(LAS u32x4*)(L + L_WO + (16 * w + i) * 1024 + lane * 16);
      const float os = o[i >> 2][i & 3];
#pragma unroll
      for (int k = 0; k < 4; ++k) { y[2 * k] = fmaf(os, bf_lo(wv[k]), y[2 * k]); y[2 * k + 1] = fmaf(os, bf_hi(wv[k]), y[2 * k + 1]); }
    }
    LAS float* yr = (LAS float*)(L + L_YR);
    *(LAS f32x4*)(yr + w * DM + lane * 8) = f32x4{y[0], y[1], y[2], y[3]};
    *(LAS f32x4*)(yr + w * DM + lane * 8 + 4) = f32x4{y[4], y[5], y[6], y[7]};
    __syncthreads();
    float ys = 0.f;
#pragma unroll
    for (int i = 0; i < NSW; ++i) ys += yr[i * DM + tid];
    st_gran((GAS u64*)(p.ws + WS_Y) + (size_t)prod * DM + tid, tag, ys);
  }
  STAMP(3);
  if (w != 0) return;
  {
    const int cb = prod >> 7, col = (prod & 127) * 4 + (lane & 3), pg = lane >> 2;
    const GAS u64* yg = (const GAS u64*)(p.ws + WS_Y) + (size_t)(cb * 128 + pg * 8) * DM + col;
    const GAS u64* mlg = (const GAS u64*)(p.ws + WS_ML) + (size_t)(cb * 128 + pg * 8 + (lane & 3)) * 2;
    const GAS u64* zg = (const GAS u64*)(p.ws + WS_Z) + (size_t)(cb * 48 + pg) * DM + col;
    const float xres = bf_lo(p.x[cb * DM + col]);
    u64 yv[8], mv[4], zv[3];
    bool good = false;
    for (unsigned spins = 0; spins < SPIN_CAP; ++spins) {
#pragma unroll
      for (int i = 0; i < 8; ++i) yv[i] = __hip_atomic_load(yg + (size_t)i * DM, RLX_AGENT);
      mv[0] = __hip_atomic_load(mlg, RLX_AGENT); mv[1] = __hip_atomic_load(mlg + 1, RLX_AGENT);
      mv[2] = __hip_atomic_load(mlg + 8, RLX_AGENT); mv[3] = __hip_atomic_load(mlg + 9, RLX_AGENT);
#pragma unroll
      for (int k = 0; k < 3; ++k) zv[k] = __hip_atomic_load(zg + (size_t)(k == 2 && pg >= NFFN - 32 ? 0 : k * 16) * DM, RLX_AGENT);
      bool ok = true;
#pragma unroll
      for (int i = 0; i < 8; ++i) ok &= (unsigned)(yv[i] >> 32) == tag;
#pragma unroll
      for (int i = 0; i < 4; ++i) ok &= (unsigned)(mv[i] >> 32) == tag;
#pragma unroll
      for (int i = 0; i < 3; ++i) ok &= (unsigned)(zv[i] >> 32) == tag;
      if (__all(ok)) { good = true; break; }
      __builtin_amdgcn_s_sleep(2);
    }
    STAMP(4);
    float wt[2];
#pragma unroll
    for (int g = 0; g < 2; ++g) {
      const float ms = gval(mv[2 * g]), ls = gval(mv[2 * g + 1]);
      float M = fmaxf(ms, dpp<XOR1>(ms)); M = fmaxf(M, dpp<XOR2>(M));
      const float e = __builtin_amdgcn_exp2f(ms - M);
      float Lh = e * ls; Lh += dpp<XOR1>(Lh); Lh += dpp<XOR2>(Lh);
      wt[g] = e / Lh;
    }
    float s = 0.f;
#pragma unroll
    for (int g = 0; g < 2; ++g) {
      s = fmaf(dpp<0x00>(wt[g]), gval(yv[4 * g + 0]), s);
      s = fmaf(dpp<0x55>(wt[g]), gval(yv[4 * g + 1]), s);
      s = fmaf(dpp<0xAA>(wt[g]), gval(yv[4 * g + 2]), s);
      s = fmaf(dpp<0xFF>(wt[g]), gval(yv[4 * g + 3]), s);
    }
    float z = gval(zv[0]) + gval(zv[1]);
    if (pg < NFFN - 32) z += gval(zv[2]);
    s += dpp<ROR4>(s); s += dpp<ROR8>(s); s = xrow_sum(s);
    z += dpp<ROR4>(z); z += dpp<ROR8>(z); z = xrow_sum(z);
    s = rbf(s); z = rbf(z);
    const float g = rbf(z / (1.0f + __expf(-z)));
    float res = xres + rbf(s * g);
    if (!good) res = __uint_as_float(0x7fc00000u);
    if (lane < 4) p.out[cb * DM + col] = (u16)bfbits(res);
    if (prod == 0) {
      const GAS u64* ml1 = (const GAS u64*)(p.ws + WS_ML) + (size_t)(128 + lane * 2) * 2;
      for (unsigned spins = 0; spins < SPIN_CAP; ++spins) {
        const u64 a0 = __hip_atomic_load(ml1, RLX_AGENT), a1 = __hip_atomic_load(ml1 + 2, RLX_AGENT);
        if (__all((unsigned)(a0 >> 32) == tag && (unsigned)(a1 >> 32) == tag)) break;
        __builtin_amdgcn_s_sleep(2);
      }
      if (lane == 0) __hip_atomic_store((GAS unsigned*)(p.ws + WS_E), tag, RLX_AGENT);
    }
    STAMP(5);
  }
}

extern "C" void kernel_launch(void* const* d_in, const int* in_sizes, int n_in, void* d_out, int out_size, void* d_ws, size_t ws_size,
                              hipStream_t stream) {
  Params p;
  p.x = (const u16*)d_in[0]; p.K = (const u16*)d_in[1]; p.V = (const u16*)d_in[2]; p.wq = (const u16*)d_in[3]; p.wo = (const u16*)d_in[4];
  p.wg = (const u16*)d_in[6]; p.wu = (const u16*)d_in[7]; p.wd = (const u16*)d_in[8];
  p.out = (u16*)d_out; p.ws = (unsigned char*)d_ws;
  fused_decode<<<NWG, NT, 0, stream>>>(p);
}
```

```cpp
#define DIAG 0
#include <hip/hip_runtime.h>
#include <hip/hip_bf16.h>
#include <stdint.h>

typedef unsigned u32x4 __attribute__((ext_vector_type(4)));
typedef float f32x4 __attribute__((ext_vector_type(4)));
typedef __bf16 bf16x2 __attribute__((ext_vector_type(2)));
typedef unsigned short u16;
typedef unsigned long long u64;
#define GAS __attribute__((address_space(1)))
#define LAS __attribute__((address_space(3)))
#define RLX_AGENT __ATOMIC_RELAXED, __HIP_MEMORY_SCOPE_AGENT

#define WO_AUX 2
#define WO_ROWS 0
#define NOFFN 0
#define EVEN_WO 1
#define WO_SUF ""
#define WO_INFL 8
constexpr int DM = 512, NH = 32, SK = 8192, DH = 128, FF = 1376;
constexpr int NWG = 256, NSW = 8, NT = (NSW + 2) * 64;
constexpr int NSPLIT = 4, KPS = SK / NSPLIT, NSTEP = KPS / (16 * NSW);
constexpr int NFFN = FF / 32;
constexpr size_t WS_E = 0, WS_ML = 4096, WS_QP = 16384, WS_GU = WS_QP + (size_t)NH * 8 * 2 * DH * 8, WS_Y = WS_GU + (size_t)48 * 4 * 128 * 8,
                 WS_Z = WS_Y + (size_t)NWG * DM * 8, WS_END = WS_Z + (size_t)2 * 48 * DM * 8;
#define STAMP(i) do {} while (0)
constexpr int L_WO = 0, L_XQ = 131072, L_Q = L_XQ + 512, L_MG = L_Q + 256, L_YR = L_MG + NSW * 132 * 4, L_TAG = L_YR + NSW * DM * 4, L_END = L_TAG + 16;
constexpr unsigned SPIN_CAP = 1u << 20;

struct Params {
  const u16 *x, *K, *V, *wq, *wo, *wg, *wu, *wd;
  u16* out;
  unsigned char* ws;
};

__device__ __forceinline__ float bf_lo(unsigned u) { return __builtin_bit_cast(float, u << 16); }
__device__ __forceinline__ float bf_hi(unsigned u) { return __builtin_bit_cast(float, u & 0xffff0000u); }
__device__ __forceinline__ float rbf(float f) { return (float)(__bf16)f; }
__device__ __forceinline__ unsigned bfbits(float f) { __bf16 b = (__bf16)f; return (unsigned)__builtin_bit_cast(u16, b); }
__device__ __forceinline__ float dot2(unsigned k, unsigned q, float acc) {
  return __builtin_amdgcn_fdot2_f32_bf16(__builtin_bit_cast(bf16x2, k), __builtin_bit_cast(bf16x2, q), acc, false);
}
template <int CTRL> __device__ __forceinline__ float dpp(float x) {
  return __builtin_bit_cast(float, __builtin_amdgcn_mov_dpp(__builtin_bit_cast(int, x), CTRL, 0xf, 0xf, true));
}
constexpr int XOR1 = 0xB1, XOR2 = 0x4E, XOR7 = 0x141, ROR8 = 0x128, ROR4 = 0x124;
__device__ __forceinline__ float xrow_sum(float x) {
  auto s = __builtin_amdgcn_permlane16_swap(__float_as_uint(x), __float_as_uint(x), false, false);
  x = __uint_as_float(s[0]) + __uint_as_float(s[1]);
  auto t = __builtin_amdgcn_permlane32_swap(__float_as_uint(x), __float_as_uint(x), false, false);
  return __uint_as_float(t[0]) + __uint_as_float(t[1]);
}
__device__ __forceinline__ float xrow_max(float x) {
  auto s = __builtin_amdgcn_permlane16_swap(__float_as_uint(x), __float_as_uint(x), false, false);
  x = fmaxf(__uint_as_float(s[0]), __uint_as_float(s[1]));
  auto t = __builtin_amdgcn_permlane32_swap(__float_as_uint(x), __float_as_uint(x), false, false);
  return fmaxf(__uint_as_float(t[0]), __uint_as_float(t[1]));
}
__device__ __forceinline__ float row_sum(float d) {
  d += dpp<XOR1>(d); d += dpp<XOR2>(d); d += dpp<XOR7>(d); d += dpp<ROR8>(d);
  return d;
}
__device__ __forceinline__ float wave_sum(float d) { return xrow_sum(row_sum(d)); }
__device__ __forceinline__ u32x4 ldnt(const GAS u32x4* p) { return __builtin_nontemporal_load(p); }
__device__ __forceinline__ void st_gran(GAS u64* g, unsigned tag, float v) {
  __hip_atomic_store(g, ((u64)tag << 32) | (u64)__float_as_uint(v), RLX_AGENT);
}
__device__ __forceinline__ float gval(u64 g) { return __uint_as_float((unsigned)g); }

__device__ __forceinline__ void ln_stats(u32x4 v, float& mb, float& den) {
  float f[8];
#pragma unroll
  for (int i = 0; i < 4; ++i) { f[2 * i] = bf_lo(v[i]); f[2 * i + 1] = bf_hi(v[i]); }
  float s = ((f[0] + f[1]) + (f[2] + f[3])) + ((f[4] + f[5]) + (f[6] + f[7]));
  s = wave_sum(s);
  const float mean = s * (1.0f / DM);
  float q = 0.f;
#pragma unroll
  for (int i = 0; i < 8; ++i) { const float d = f[i] - mean; q = fmaf(d, d, q); }
  q = wave_sum(q);
  const float var = rbf(q * (1.0f / DM));
  mb = rbf(mean);
  den = rbf(sqrtf(rbf(var + 1e-5f)));
}
__device__ __forceinline__ float ln_apply(float x, float mb, float den) { return rbf(rbf(x - mb) / den); }

__device__ __forceinline__ float gelu_tanh(float x) {
  const float inner = 0.7978845608028654f * (x + 0.044715f * x * x * x);
  return 0.5f * x * (1.0f + tanhf(inner));
}

__global__ void __launch_bounds__(NT) fused_decode(Params p) {
  __shared__ __attribute__((aligned(16))) unsigned char smem[L_END];
  LAS unsigned char* L = (LAS unsigned char*)smem;
  const int tid = threadIdx.x, lane = tid & 63, w = __builtin_amdgcn_readfirstlane(tid >> 6);
  const int bid = blockIdx.x, xg = bid & 7, rk = bid >> 3;
  const int h = xg * 4 + (rk >> 3), b = (rk >> 2) & 1, sp = rk & 3;
  const int prod = b * 128 + h * 4 + sp;
  const int r = lane >> 4, c = lane & 15;
  const unsigned dg_t0 = (unsigned)__builtin_amdgcn_s_memrealtime();
  if (w == NSW) {
    __builtin_amdgcn_s_setprio(3);
    const int i8 = rk & 7;
    const unsigned e0 = __hip_atomic_load((GAS unsigned*)(p.ws + WS_E), RLX_AGENT);
    const u32x4 x0 = *(const GAS u32x4*)(p.x + lane * 8), x1 = *(const GAS u32x4*)(p.x + DM + lane * 8);
    const unsigned xa = p.x[64 * i8 + lane], xb = p.x[DM + 64 * i8 + lane];
    u32x4 wqv[16];
    {
      const GAS u16* src = (const GAS u16*)p.wq + (size_t)(64 * i8 + 16 * r) * (NH * DH) + h * DH + c * 8;
#pragma unroll
      for (int i = 0; i < 16; ++i) wqv[i] = ldnt((const GAS u32x4*)(src + (size_t)i * (NH * DH)));
    }
    __builtin_amdgcn_sched_barrier(0);
    const unsigned tag = e0 + 1u;
    float m0, d0, m1, d1;
    ln_stats(x0, m0, d0); ln_stats(x1, m1, d1);
    LAS float* xq = (LAS float*)(L + L_XQ);
    xq[lane] = ln_apply(bf_lo(xa), m0, d0);
    xq[64 + lane] = ln_apply(bf_lo(xb), m1, d1);
    if (lane == 0) *(volatile LAS unsigned*)(L + L_TAG) = tag;
    f32x4 xr0[4], xr1[4];
#pragma unroll
    for (int i = 0; i < 4; ++i) { xr0[i] = *(LAS f32x4*)(xq + 16 * r + 4 * i); xr1[i] = *(LAS f32x4*)(xq + 64 + 16 * r + 4 * i); }
    float a0[8], a1[8];
#pragma unroll
    for (int i = 0; i < 8; ++i) a0[i] = a1[i] = 0.f;
#pragma unroll
    for (int i = 0; i < 16; ++i) {
      const float s0 = xr0[i >> 2][i & 3], s1 = xr1[i >> 2][i & 3];
#pragma unroll
      for (int k = 0; k < 4; ++k) {
        const float lo = bf_lo(wqv[i][k]), hi = bf_hi(wqv[i][k]);
        a0[2 * k] = fmaf(s0, lo, a0[2 * k]); a0[2 * k + 1] = fmaf(s0, hi, a0[2 * k + 1]);
        a1[2 * k] = fmaf(s1, lo, a1[2 * k]); a1[2 * k + 1] = fmaf(s1, hi, a1[2 * k + 1]);
      }
    }
#pragma unroll
    for (int i = 0; i < 8; ++i) { a0[i] = xrow_sum(a0[i]); a1[i] = xrow_sum(a1[i]); }
    {
      GAS u64* qp = (GAS u64*)(p.ws + WS_QP) + (size_t)((h * 8 + i8) * 2 + (r >> 1)) * DH + c * 8 + (r & 1) * 4;
#pragma unroll
      for (int k = 0; k < 4; ++k) {
        const float t0 = a0[k], t1 = a0[4 + k], t2 = a1[k], t3 = a1[4 + k];
        const float lo = (r & 1) ? t1 : t0, hi = (r & 1) ? t3 : t2;
        st_gran(qp + k, tag, (r & 2) ? hi : lo);
      }
    }
    STAMP(1);
    {
      const GAS u64* qg = (const GAS u64*)(p.ws + WS_QP) + (size_t)(h * 8 * 2 + b) * DH + lane;
      u64 g[16];
      for (unsigned spins = 0; spins < SPIN_CAP; ++spins) {
#pragma unroll
        for (int i = 0; i < 8; ++i) { g[2 * i] = __hip_atomic_load(qg + i * 2 * DH, RLX_AGENT); g[2 * i + 1] = __hip_atomic_load(qg + i * 2 * DH + 64, RLX_AGENT); }
        bool ok = true;
#pragma unroll
        for (int i = 0; i < 16; ++i) ok &= (unsigned)(g[i] >> 32) == tag;
        if (__all(ok)) break;
      }
      float q0 = 0.f, q1 = 0.f;
#pragma unroll
      for (int i = 0; i < 8; ++i) { q0 += gval(g[2 * i]); q1 += gval(g[2 * i + 1]); }
      LAS u16* ql = (LAS u16*)(L + L_Q);
      ql[lane] = (u16)bfbits(q0); ql[64 + lane] = (u16)bfbits(q1);
    }
    STAMP(2);
    __syncthreads();
    {
      const GAS u16* src = (const GAS u16*)p.wo + (size_t)h * DH * DM + lane * 8;
#pragma unroll 1
      for (int i0 = 0; i0 < WO_ROWS; i0 += 4) {
#pragma unroll
        for (int i = i0; i < i0 + 4; ++i)
          __builtin_amdgcn_global_load_lds((const GAS unsigned*)(src + i * DM), (LAS unsigned*)(L + L_WO + i * 1024), 16, 0, WO_AUX);
        asm volatile("s_waitcnt vmcnt(%0)" :: "i"(WO_INFL) : "memory");
      }
    }
    asm volatile("s_waitcnt vmcnt(0)" ::: "memory");
    STAMP(3);
    return;
  }
  if (w == NSW + 1) {
    __builtin_amdgcn_s_setprio(2);
    __syncthreads();
    const unsigned tag = *(volatile LAS unsigned*)(L + L_TAG);
    LAS float* sc = (LAS float*)(L + L_YR);
    if (NOFFN) {
    } else if (rk < 22) {
      const int j = (xg & 1) * 22 + rk, rq = xg >> 1;
      if (j < NFFN) {
        const int rg = lane >> 2, c4 = lane & 3;
        const u32x4 x0 = *(const GAS u32x4*)(p.x + lane * 8), x1 = *(const GAS u32x4*)(p.x + DM + lane * 8);
        const unsigned xa = *(const GAS unsigned*)(p.x + 128 * rq + 2 * lane), xb = *(const GAS unsigned*)(p.x + DM + 128 * rq + 2 * lane);
        const GAS u16* gp = (const GAS u16*)p.wg + (size_t)(128 * rq + rg) * FF + j * 32 + c4 * 8;
        const GAS u16* up = (const GAS u16*)p.wu + (size_t)(128 * rq + rg) * FF + j * 32 + c4 * 8;
        u32x4 g[8], u[8];
#pragma unroll
        for (int i = 0; i < 8; ++i) { g[i] = ldnt((const GAS u32x4*)(gp + (size_t)i * 16 * FF)); u[i] = ldnt((const GAS u32x4*)(up + (size_t)i * 16 * FF)); }
        float m0, d0, m1, d1;
        ln_stats(x0, m0, d0); ln_stats(x1, m1, d1);
        sc[2 * lane] = ln_apply(bf_lo(xa), m0, d0); sc[2 * lane + 1] = ln_apply(bf_hi(xa), m0, d0);
        sc[128 + 2 * lane] = ln_apply(bf_lo(xb), m1, d1); sc[128 + 2 * lane + 1] = ln_apply(bf_hi(xb), m1, d1);
        float ag[2][8], au[2][8];
#pragma unroll
        for (int i = 0; i < 8; ++i) { ag[0][i] = ag[1][i] = au[0][i] = au[1][i] = 0.f; }
#pragma unroll
        for (int i = 0; i < 8; ++i) {
          const float a0 = sc[i * 16 + rg], a1 = sc[128 + i * 16 + rg];
#pragma unroll
          for (int k = 0; k < 4; ++k) {
            const float gl = bf_lo(g[i][k]), gh = bf_hi(g[i][k]), ul = bf_lo(u[i][k]), uh = bf_hi(u[i][k]);
            ag[0][2 * k] = fmaf(a0, gl, ag[0][2 * k]); ag[0][2 * k + 1] = fmaf(a0, gh, ag[0][2 * k + 1]);
            ag[1][2 * k] = fmaf(a1, gl, ag[1][2 * k]); ag[1][2 * k + 1] = fmaf(a1, gh, ag[1][2 * k + 1]);
            au[0][2 * k] = fmaf(a0, ul, au[0][2 * k]); au[0][2 * k + 1] = fmaf(a0, uh, au[0][2 * k + 1]);
            au[1][2 * k] = fmaf(a1, ul, au[1][2 * k]); au[1][2 * k + 1] = fmaf(a1, uh, au[1][2 * k + 1]);
          }
        }
#pragma unroll
        for (int bb = 0; bb < 2; ++bb)
#pragma unroll
          for (int i = 0; i < 8; ++i) {
            float gs = ag[bb][i], us = au[bb][i];
            gs += dpp<ROR4>(gs); gs += dpp<ROR8>(gs); gs = xrow_sum(gs);
            us += dpp<ROR4>(us); us += dpp<ROR8>(us); us = xrow_sum(us);
            if (rg == 0) { sc[256 + bb * 32 + c4 * 8 + i] = gs; sc[320 + bb * 32 + c4 * 8 + i] = us; }
          }
        GAS u64* gu = (GAS u64*)(p.ws + WS_GU) + (size_t)(j * 4 + rq) * 128 + lane;
        st_gran(gu, tag, sc[256 + lane]); st_gran(gu + 64, tag, sc[320 + lane]);
      }
    } else if (rk < 28) {
      const int j = xg * 6 + (rk - 22);
      if (j < NFFN) {
        const GAS u64* gu = (const GAS u64*)(p.ws + WS_GU) + (size_t)j * 4 * 128 + lane;
        u64 g[8];
        for (unsigned spins = 0; spins < SPIN_CAP; ++spins) {
          __builtin_amdgcn_s_sleep(32);
#pragma unroll
          for (int i = 0; i < 8; ++i) g[i] = __hip_atomic_load(gu + i * 64, RLX_AGENT);
          bool ok = true;
#pragma unroll
          for (int i = 0; i < 8; ++i) ok &= (unsigned)(g[i] >> 32) == tag;
          if (__all(ok)) break;
        }
        const float gs = rbf((gval(g[0]) + gval(g[2])) + (gval(g[4]) + gval(g[6]))), us = rbf((gval(g[1]) + gval(g[3])) + (gval(g[5]) + gval(g[7])));
        sc[lane] = rbf(rbf(gelu_tanh(gs)) * us);
        float z[2][8];
#pragma unroll
        for (int i = 0; i < 8; ++i) z[0][i] = z[1][i] = 0.f;
        const GAS u16* dp = (const GAS u16*)p.wd + (size_t)j * 32 * DM + lane * 8;
#pragma unroll 1
        for (int it = 0; it < 4; ++it) {
          u32x4 d[8];
#pragma unroll
          for (int i = 0; i < 8; ++i) d[i] = ldnt((const GAS u32x4*)(dp + (it * 8 + i) * DM));
#pragma unroll
          for (int i = 0; i < 8; ++i) {
            const float a0 = sc[it * 8 + i], a1 = sc[32 + it * 8 + i];
#pragma unroll
            for (int k = 0; k < 4; ++k) {
              const float lo = bf_lo(d[i][k]), hi = bf_hi(d[i][k]);
              z[0][2 * k] = fmaf(a0, lo, z[0][2 * k]); z[0][2 * k + 1] = fmaf(a0, hi, z[0][2 * k + 1]);
              z[1][2 * k] = fmaf(a1, lo, z[1][2 * k]); z[1][2 * k + 1] = fmaf(a1, hi, z[1][2 * k + 1]);
            }
          }
        }
#pragma unroll
        for (int bb = 0; bb < 2; ++bb) {
          *(LAS f32x4*)(sc + 64 + bb * DM + lane * 8) = f32x4{z[bb][0], z[bb][1], z[bb][2], z[bb][3]};
          *(LAS f32x4*)(sc + 64 + bb * DM + lane * 8 + 4) = f32x4{z[bb][4], z[bb][5], z[bb][6], z[bb][7]};
        }
        GAS u64* zg = (GAS u64*)(p.ws + WS_Z);
#pragma unroll
        for (int bb = 0; bb < 2; ++bb)
#pragma unroll
          for (int i = 0; i < 8; ++i) st_gran(zg + (size_t)(bb * 48 + j) * DM + i * 64 + lane, tag, sc[64 + bb * DM + i * 64 + lane]);
      }
    }
    STAMP(4);
    return;
  }
  const size_t kvbase = ((size_t)(b * NH + h) * SK + (size_t)sp * KPS) * DH / 8 + (size_t)w * 256 + lane;
  const GAS u32x4* kp = (const GAS u32x4*)p.K + kvbase;
  const GAS u32x4* vp = (const GAS u32x4*)p.V + kvbase;
  u32x4 kk[2][4], vv[2][4];
#pragma unroll
  for (int d = 0; d < 2; ++d) {
#pragma unroll
    for (int u = 0; u < 4; ++u) kk[d][u] = ldnt(kp + d * (NSW * 256) + u * 64);
#pragma unroll
    for (int u = 0; u < 4; ++u) vv[d][u] = ldnt(vp + d * (NSW * 256) + u * 64);
  }
  const GAS u16* wosrc = (const GAS u16*)p.wo + (size_t)(h * DH + w * 16) * DM + lane * 8;
  unsigned wodst = (unsigned)(size_t)(L + L_WO) + w * 16 * 1024;
#define WODMA() do { if (EVEN_WO) { unsigned keep; asm volatile("s_mov_b32 %0, m0\n\ts_mov_b32 m0, %2\n\ts_nop 0\n\tglobal_load_lds_dwordx4 %1, off" WO_SUF "\n\ts_mov_b32 m0, %0" : "=&s"(keep) : "v"(wosrc), "s"(wodst) : "memory"); wosrc += DM; wodst += 1024; } } while (0)
  WODMA(); WODMA();
  __builtin_amdgcn_sched_barrier(0);
  STAMP(6);
  __syncthreads();
  const u32x4 qv = *(LAS u32x4*)(L + L_Q + c * 16);
  const unsigned tag = *(volatile LAS unsigned*)(L + L_TAG);
  const unsigned dg_tq = (unsigned)__builtin_amdgcn_s_memrealtime();
  float m = -INFINITY, l = 0.f, acc[8];
#pragma unroll
  for (int i = 0; i < 8; ++i) acc[i] = 0.f;
  constexpr float SCALE = 0.08838834764831845f, LOG2E = 1.4426950408889634f;
#define STEP(D, REFILL, OFF)                                                                      \
  {                                                                                               \
    float sc[4];                                                                                  \
    _Pragma("unroll") for (int u = 0; u < 4; ++u) {                                               \
      float d = dot2(kk[D][u][0], qv[0], 0.f);                                                    \
      d = dot2(kk[D][u][1], qv[1], d); d = dot2(kk[D][u][2], qv[2], d); d = dot2(kk[D][u][3], qv[3], d); \
      sc[u] = d;                                                                                  \
    }                                                                                             \
    __builtin_amdgcn_sched_barrier(0);                                                            \
    if (REFILL) { _Pragma("unroll") for (int u = 0; u < 4; ++u) kk[D][u] = ldnt(kp + (OFF) + u * 64); WODMA(); } \
    __builtin_amdgcn_sched_barrier(0);                                                            \
    _Pragma("unroll") for (int u = 0; u < 4; ++u) sc[u] = rbf(rbf(row_sum(sc[u])) * SCALE) * LOG2E; \
    const float mx = fmaxf(fmaxf(m, fmaxf(sc[0], sc[1])), fmaxf(sc[2], sc[3]));                   \
    const float al = __builtin_amdgcn_exp2f(m - mx);                                              \
    m = mx;                                                                                       \
    float pp[4];                                                                                  \
    _Pragma("unroll") for (int u = 0; u < 4; ++u) pp[u] = __builtin_amdgcn_exp2f(sc[u] - mx);     \
    l = fmaf(l, al, (pp[0] + pp[1]) + (pp[2] + pp[3]));                                           \
    _Pragma("unroll") for (int i = 0; i < 8; ++i) acc[i] *= al;                                   \
    _Pragma("unroll") for (int u = 0; u < 4; ++u)                                                 \
      _Pragma("unroll") for (int k = 0; k < 4; ++k) {                                             \
        acc[2 * k] = fmaf(pp[u], bf_lo(vv[D][u][k]), acc[2 * k]);                                 \
        acc[2 * k + 1] = fmaf(pp[u], bf_hi(vv[D][u][k]), acc[2 * k + 1]);                         \
      }                                                                                           \
    __builtin_amdgcn_sched_barrier(0);                                                            \
    if (REFILL) { _Pragma("unroll") for (int u = 0; u < 4; ++u) vv[D][u] = ldnt(vp + (OFF) + u * 64); } \
    __builtin_amdgcn_sched_barrier(0);                                                            \
  }
#pragma unroll 1
  for (int s = 2; s < NSTEP; s += 2) {
    STEP(0, true, s * (NSW * 256))
    STEP(1, true, (s + 1) * (NSW * 256))
  }
  STEP(0, false, 0)
  STEP(1, false, 0)
#undef STEP
  STAMP(2);
  {
    const float M = xrow_max(m);
    const float f = __builtin_amdgcn_exp2f(m - M);
    l = xrow_sum(l * f);
#pragma unroll
    for (int i = 0; i < 8; ++i) acc[i] = xrow_sum(acc[i] * f);
    LAS float* mg = (LAS float*)(L + L_MG) + w * 132;
    if (r == 0) {
      *(LAS f32x4*)(mg + 4 + c * 8) = f32x4{acc[0], acc[1], acc[2], acc[3]};
      *(LAS f32x4*)(mg + 8 + c * 8) = f32x4{acc[4], acc[5], acc[6], acc[7]};
      if (c == 0) { mg[0] = M; mg[1] = l; }
    }
  }
  __syncthreads();
  const unsigned dg_tb = (unsigned)__builtin_amdgcn_s_memrealtime();
  if (tid == 0) { GAS u64* dg = (GAS u64*)(p.ws + WS_END) + prod * 4; st_gran(dg, tag, __uint_as_float(dg_t0)); st_gran(dg + 1, tag, __uint_as_float(dg_tq)); st_gran(dg + 2, tag, __uint_as_float(dg_tb)); }
  {
    LAS float* mg = (LAS float*)(L + L_MG);
    float mw[NSW], M = -INFINITY;
#pragma unroll
    for (int i = 0; i < NSW; ++i) { mw[i] = mg[i * 132]; M = fmaxf(M, mw[i]); }
    float Ls = 0.f;
    f32x4 o[4];
#pragma unroll
    for (int k = 0; k < 4; ++k) o[k] = f32x4{0.f, 0.f, 0.f, 0.f};
#pragma unroll
    for (int i = 0; i < NSW; ++i) {
      const float f = __builtin_amdgcn_exp2f(mw[i] - M);
      Ls = fmaf(f, mg[i * 132 + 1], Ls);
#pragma unroll
      for (int k = 0; k < 4; ++k) o[k] += f * *(LAS f32x4*)(mg + i * 132 + 4 + 16 * w + 4 * k);
    }
    if (tid == 0) {
      GAS u64* ml = (GAS u64*)(p.ws + WS_ML) + prod * 2;
      st_gran(ml, tag, M); st_gran(ml + 1, tag, Ls);
    }
    float y[8];
#pragma unroll
    for (int i = 0; i < 8; ++i) y[i] = 0.f;
#pragma unroll
    for (int i = 0; i < 16; ++i) {
      const u32x4 wv = *(LAS u32x4*)(L + L_WO + (16 * w + i) * 1024 + lane * 16);
      const float os = o[i >> 2][i & 3];
#pragma unroll
      for (int k = 0; k < 4; ++k) { y[2 * k] = fmaf(os, bf_lo(wv[k]), y[2 * k]); y[2 * k + 1] = fmaf(os, bf_hi(wv[k]), y[2 * k + 1]); }
    }
    LAS float* yr = (LAS float*)(L + L_YR);
    *(LAS f32x4*)(yr + w * DM + lane * 8) = f32x4{y[0], y[1], y[2], y[3]};
    *(LAS f32x4*)(yr + w * DM + lane * 8 + 4) = f32x4{y[4], y[5], y[6], y[7]};
    __syncthreads();
    float ys = 0.f;
#pragma unroll
    for (int i = 0; i < NSW; ++i) ys += yr[i * DM + tid];
    st_gran((GAS u64*)(p.ws + WS_Y) + (size_t)prod * DM + tid, tag, ys);
  }
  STAMP(3);
  if (w != 0) return;
  {
    const int cb = prod >> 7, col = (prod & 127) * 4 + (lane & 3), pg = lane >> 2;
    const GAS u64* yg = (const GAS u64*)(p.ws + WS_Y) + (size_t)(cb * 128 + pg * 8) * DM + col;
    const GAS u64* mlg = (const GAS u64*)(p.ws + WS_ML) + (size_t)(cb * 128 + pg * 8 + (lane & 3)) * 2;
    const GAS u64* zg = (const GAS u64*)(p.ws + WS_Z) + (size_t)(cb * 48 + pg) * DM + col;
    const float xres = bf_lo(p.x[cb * DM + col]);
    u64 yv[8], mv[4], zv[3];
    bool good = false;
    for (unsigned spins = 0; spins < SPIN_CAP; ++spins) {
#pragma unroll
      for (int i = 0; i < 8; ++i) yv[i] = __hip_atomic_load(yg + (size_t)i * DM, RLX_AGENT);
      mv[0] = __hip_atomic_load(mlg, RLX_AGENT); mv[1] = __hip_atomic_load(mlg + 1, RLX_AGENT);
      mv[2] = __hip_atomic_load(mlg + 8, RLX_AGENT); mv[3] = __hip_atomic_load(mlg + 9, RLX_AGENT);
#pragma unroll
      for (int k = 0; k < 3; ++k) zv[k] = __hip_atomic_load(zg + (size_t)(k == 2 && pg >= NFFN - 32 ? 0 : k * 16) * DM, RLX_AGENT);
      bool ok = true;
#pragma unroll
      for (int i = 0; i < 8; ++i) ok &= (unsigned)(yv[i] >> 32) == tag;
#pragma unroll
      for (int i = 0; i < 4; ++i) ok &= (unsigned)(mv[i] >> 32) == tag;
#pragma unroll
      for (int i = 0; i < 3; ++i) ok &= NOFFN || (unsigned)(zv[i] >> 32) == tag;
      if (__all(ok)) { good = true; break; }
      __builtin_amdgcn_s_sleep(2);
    }
    STAMP(4);
    float wt[2];
#pragma unroll
    for (int g = 0; g < 2; ++g) {
      const float ms = gval(mv[2 * g]), ls = gval(mv[2 * g + 1]);
      float M = fmaxf(ms, dpp<XOR1>(ms)); M = fmaxf(M, dpp<XOR2>(M));
      const float e = __builtin_amdgcn_exp2f(ms - M);
      float Lh = e * ls; Lh += dpp<XOR1>(Lh); Lh += dpp<XOR2>(Lh);
      wt[g] = e / Lh;
    }
    float s = 0.f;
#pragma unroll
    for (int g = 0; g < 2; ++g) {
      s = fmaf(dpp<0x00>(wt[g]), gval(yv[4 * g + 0]), s);
      s = fmaf(dpp<0x55>(wt[g]), gval(yv[4 * g + 1]), s);
      s = fmaf(dpp<0xAA>(wt[g]), gval(yv[4 * g + 2]), s);
      s = fmaf(dpp<0xFF>(wt[g]), gval(yv[4 * g + 3]), s);
    }
    float z = gval(zv[0]) + gval(zv[1]);
    if (pg < NFFN - 32) z += gval(zv[2]);
    s += dpp<ROR4>(s); s += dpp<ROR8>(s); s = xrow_sum(s);
    z += dpp<ROR4>(z); z += dpp<ROR8>(z); z = xrow_sum(z);
    s = rbf(s); z = rbf(z);
    const float g = rbf(z / (1.0f + __expf(-z)));
    float res = xres + rbf(s * g);
    if (!good) res = __uint_as_float(0x7fc00000u);
    if (lane < 4) p.out[cb * DM + col] = (u16)bfbits(res);
    if (prod == 0) {
      const GAS u64* ml1 = (const GAS u64*)(p.ws + WS_ML) + (size_t)(128 + lane * 2) * 2;
      for (unsigned spins = 0; spins < SPIN_CAP; ++spins) {
        const u64 a0 = __hip_atomic_load(ml1, RLX_AGENT), a1 = __hip_atomic_load(ml1 + 2, RLX_AGENT);
        if (__all((unsigned)(a0 >> 32) == tag && (unsigned)(a1 >> 32) == tag)) break;
        __builtin_amdgcn_s_sleep(2);
      }
      {
        const unsigned tnow = (unsigned)__builtin_amdgcn_s_memrealtime();
        const GAS u64* dg = (const GAS u64*)(p.ws + WS_END) + lane * 16;
        u64 g[4][3];
        for (unsigned spins = 0; spins < SPIN_CAP; ++spins) {
          bool ok = true;
          for (int i = 0; i < 4; ++i) for (int k = 0; k < 3; ++k) { g[i][k] = __hip_atomic_load(dg + i * 4 + k, RLX_AGENT); ok &= (unsigned)(g[i][k] >> 32) == tag; }
          if (__all(ok)) break;
        }
        int mn[3], mx[3], sm[3];
        for (int k = 0; k < 3; ++k) { mn[k] = 1 << 30; mx[k] = 0; sm[k] = 0; for (int i = 0; i < 4; ++i) { int a = (int)(tnow - (unsigned)g[i][k]); mn[k] = min(mn[k], a); mx[k] = max(mx[k], a); sm[k] += a; }
          for (int o = 32; o > 0; o >>= 1) { mn[k] = min(mn[k], __shfl_xor(mn[k], o)); mx[k] = max(mx[k], __shfl_xor(mx[k], o)); sm[k] += __shfl_xor(sm[k], o); } sm[k] /= 256; }
        const int T0 = mx[0];
        int X = 0;
        if (DIAG == 1) X = T0;
        if (DIAG == 2) X = (T0 - mn[0]) * 10;
        if (DIAG == 3) X = (T0 - sm[1]) * 4;
        if (DIAG == 4) X = (T0 - sm[2]);
        if (DIAG == 5) X = (T0 - mx[2]);
        if (DIAG == 6) X = (T0 - mn[2]);
        if (DIAG == 7) X = (T0 - sm[0]) * 10;
        X = __builtin_amdgcn_readfirstlane(X);
        while ((int)((unsigned)__builtin_amdgcn_s_memrealtime() - tnow) < X) __builtin_amdgcn_s_sleep(1);
      }
      if (lane == 0) __hip_atomic_store((GAS unsigned*)(p.ws + WS_E), tag, RLX_AGENT);
    }
    STAMP(5);
  }
}

extern "C" void kernel_launch(void* const* d_in, const int* in_sizes, int n_in, void* d_out, int out_size, void* d_ws, size_t ws_size,
                              hipStream_t stream) {
  Params p;
  p.x = (const u16*)d_in[0]; p.K = (const u16*)d_in[1]; p.V = (const u16*)d_in[2]; p.wq = (const u16*)d_in[3]; p.wo = (const u16*)d_in[4];
  p.wg = (const u16*)d_in[6]; p.wu = (const u16*)d_in[7]; p.wd = (const u16*)d_in[8];
  p.out = (u16*)d_out; p.ws = (unsigned char*)d_ws;
  fused_decode<<<NWG, NT, 0, stream>>>(p);
}
```

```cpp
#include <hip/hip_runtime.h>
#include <hip/hip_bf16.h>
#include <stdint.h>

typedef unsigned u32x4 __attribute__((ext_vector_type(4)));
typedef unsigned u32x2 __attribute__((ext_vector_type(2)));
typedef float f32x4 __attribute__((ext_vector_type(4)));
typedef float f32x2 __attribute__((ext_vector_type(2)));
typedef __bf16 bf16x2 __attribute__((ext_vector_type(2)));
typedef __bf16 bf16x8 __attribute__((ext_vector_type(8)));
typedef unsigned short u16;
typedef unsigned long long u64;
#define GAS __attribute__((address_space(1)))
#define LAS __attribute__((address_space(3)))
#define RLX_AGENT __ATOMIC_RELAXED, __HIP_MEMORY_SCOPE_AGENT

#define PACE 3900
#define PACE0 0
#define WO_T0 900
#define WO_DT 80
#define NEXTRA 4
#define C_WAIT 36
#define AUX_FIRST 1
#define WO_MODE 1
#define W_NT 1
#define NOFFN 0
#define CODE_PF 2
#define CODE_LINES 144
#define CPL ((CODE_LINES + 31) / 32)
#define Q_WAIT 16
#define PRO_THROTTLE 11
#define K_AUX 2
#define V_AUX 2
constexpr int DM = 512, NH = 32, SK = 8192, DH = 128, FF = 1376;
constexpr int NWG = 256, NAW = 4, NSW = 4, NT = (NAW + NSW) * 64;
constexpr int NSPLIT = 4, KPS = SK / NSPLIT, NSTEP = KPS / (16 * NSW), NX = NEXTRA;
constexpr int STEPB = NSW * 4096;
constexpr int NSL = FF / 32, NZG = 9;
constexpr size_t WS_E = 0, WS_QP = 4096, WS_GU = WS_QP + (size_t)NH * 8 * 64 * 8, WS_Z = WS_GU + (size_t)45 * 4 * 64 * 8,
                 WS_Y = WS_Z + (size_t)NZG * 8 * 128 * 8, WS_END = WS_Y + (size_t)NWG * DM * 8;
#define STAMP(i) do {} while (0)
#define STAMP_OUT() do {} while (0)
constexpr int REC_STRIDE = 272;
constexpr int L_Q = 0, L_REC = 256, L_MLW = L_REC + 8 * REC_STRIDE, L_G = L_MLW + 32, L_CT = L_G + 128, L_RED = L_CT + 16, L_PQ = L_RED + NSW * 64,
              L_PF = L_PQ + NAW * 256, L_H = L_PF + NAW * 512, L_X = 8192, L_END = L_X + NX * NSW * 8192;
static_assert(L_H + NAW * 512 <= L_X, "");
constexpr unsigned SPIN_CAP = 1u << 20;
constexpr int OOB = 0x7ffffff0;

struct Params {
  const u16 *x, *K, *V, *wq, *wo, *wg, *wu, *wd;
  u16* out;
  unsigned char* ws;
};

__device__ __forceinline__ float bf_lo(unsigned u) { return __builtin_bit_cast(float, u << 16); }
__device__ __forceinline__ float bf_hi(unsigned u) { return __builtin_bit_cast(float, u & 0xffff0000u); }
__device__ __forceinline__ float rbf(float f) { return (float)(__bf16)f; }
__device__ __forceinline__ unsigned bfbits(float f) { __bf16 b = (__bf16)f; return (unsigned)__builtin_bit_cast(u16, b); }
__device__ __forceinline__ unsigned pkbf(float lo, float hi) { const bf16x2 v = {(__bf16)lo, (__bf16)hi}; return __builtin_bit_cast(unsigned, v); }
__device__ __forceinline__ float dot2(unsigned k, unsigned q, float acc) {
  return __builtin_amdgcn_fdot2_f32_bf16(__builtin_bit_cast(bf16x2, k), __builtin_bit_cast(bf16x2, q), acc, false);
}
template <int DC> __device__ __forceinline__ float dpp(float x) {
  return __builtin_bit_cast(float, __builtin_amdgcn_mov_dpp(__builtin_bit_cast(int, x), DC, 0xf, 0xf, true));
}
constexpr int XOR1 = 0xB1, XOR2 = 0x4E, XOR7 = 0x141, ROR8 = 0x128, ROR4 = 0x124;
__device__ __forceinline__ float swap16_sum(float a, float b) {
  auto s = __builtin_amdgcn_permlane16_swap(__float_as_uint(a), __float_as_uint(b), false, false);
  return __uint_as_float(s[0]) + __uint_as_float(s[1]);
}
__device__ __forceinline__ float swap32_sum(float a, float b) {
  auto s = __builtin_amdgcn_permlane32_swap(__float_as_uint(a), __float_as_uint(b), false, false);
  return __uint_as_float(s[0]) + __uint_as_float(s[1]);
}
__device__ __forceinline__ float xrow_sum(float x) { x = swap16_sum(x, x); return swap32_sum(x, x); }
__device__ __forceinline__ float xrow_max(float x) {
  auto s = __builtin_amdgcn_permlane16_swap(__float_as_uint(x), __float_as_uint(x), false, false);
  x = fmaxf(__uint_as_float(s[0]), __uint_as_float(s[1]));
  auto t = __builtin_amdgcn_permlane32_swap(__float_as_uint(x), __float_as_uint(x), false, false);
  return fmaxf(__uint_as_float(t[0]), __uint_as_float(t[1]));
}
__device__ __forceinline__ float row_sum(float d) {
  d += dpp<XOR1>(d); d += dpp<XOR2>(d); d += dpp<XOR7>(d); d += dpp<ROR8>(d);
  return d;
}
__device__ __forceinline__ float wave_sum(float d) { return xrow_sum(row_sum(d)); }
__device__ __forceinline__ void st_gran2(__amdgpu_buffer_rsrc_t rs, int off, unsigned tag, float a, float b) {
  __builtin_amdgcn_raw_buffer_store_b128(u32x4{__float_as_uint(a), tag, __float_as_uint(b), tag}, rs, off, 0, 16);
}
__device__ __forceinline__ u32x4 ld_gran2(__amdgpu_buffer_rsrc_t rs, int off) { return __builtin_amdgcn_raw_buffer_load_b128(rs, off, 0, 16); }
__device__ __forceinline__ u32x2 ld_gran(__amdgpu_buffer_rsrc_t rs, int off) { return __builtin_amdgcn_raw_buffer_load_b64(rs, off, 0, 16); }
__device__ __forceinline__ f32x4 mfma16(u32x4 a, u32x4 b, f32x4 c) {
  return __builtin_amdgcn_mfma_f32_16x16x32_bf16(__builtin_bit_cast(bf16x8, a), __builtin_bit_cast(bf16x8, b), c, 0, 0, 0);
}
__device__ __forceinline__ unsigned now32() { return (unsigned)__builtin_amdgcn_s_memrealtime(); }
__device__ __forceinline__ void wait_until(unsigned t0, unsigned due) { while (now32() - t0 < due) __builtin_amdgcn_s_sleep(2); }
__device__ __forceinline__ bool lds_arrive_last(LAS unsigned char* L, int idx) {
  unsigned old = 0;
  if ((threadIdx.x & 63) == 0) old = __hip_atomic_fetch_add((LAS unsigned*)(L + L_CT) + idx, 1u, __ATOMIC_ACQ_REL, __HIP_MEMORY_SCOPE_WORKGROUP);
  return __builtin_amdgcn_readfirstlane(old) == NAW - 1;
}

__device__ __forceinline__ void ln_stats(u32x4 v, float& mb, float& den) {
  float f[8];
#pragma unroll
  for (int i = 0; i < 4; ++i) { f[2 * i] = bf_lo(v[i]); f[2 * i + 1] = bf_hi(v[i]); }
  float s = ((f[0] + f[1]) + (f[2] + f[3])) + ((f[4] + f[5]) + (f[6] + f[7]));
  s = wave_sum(s);
  const float mean = s * (1.0f / DM);
  float q = 0.f;
#pragma unroll
  for (int i = 0; i < 8; ++i) { const float d = f[i] - mean; q = fmaf(d, d, q); }
  q = wave_sum(q);
  const float var = rbf(q * (1.0f / DM));
  mb = rbf(mean);
  den = rbf(sqrtf(rbf(var + 1e-5f)));
}
__device__ __forceinline__ float ln_apply(float x, float mb, float den) { return rbf(rbf(x - mb) / den); }
__device__ __forceinline__ float gelu_tanh(float x) {
  const float inner = 0.7978845608028654f * (x + 0.044715f * x * x * x);
  return 0.5f * x * (1.0f + tanhf(inner));
}
__device__ __forceinline__ float gemv64x32(const u32x4 (&wv)[4], float n0, float n1, int lane) {
  const float s0[4] = {dpp<0x00>(n0), dpp<0x55>(n0), dpp<0xAA>(n0), dpp<0xFF>(n0)};
  const float s1[4] = {dpp<0x00>(n1), dpp<0x55>(n1), dpp<0xAA>(n1), dpp<0xFF>(n1)};
  float a0[8], a1[8];
#pragma unroll
  for (int i = 0; i < 8; ++i) a0[i] = a1[i] = 0.f;
#pragma unroll
  for (int i = 0; i < 4; ++i)
#pragma unroll
    for (int k = 0; k < 4; ++k) {
      const float lo = bf_lo(wv[i][k]), hi = bf_hi(wv[i][k]);
      a0[2 * k] = fmaf(s0[i], lo, a0[2 * k]); a0[2 * k + 1] = fmaf(s0[i], hi, a0[2 * k + 1]);
      a1[2 * k] = fmaf(s1[i], lo, a1[2 * k]); a1[2 * k + 1] = fmaf(s1[i], hi, a1[2 * k + 1]);
    }
  float v[8], t[4];
#pragma unroll
  for (int i = 0; i < 8; ++i) v[i] = swap32_sum(a0[i], a1[i]);
#pragma unroll
  for (int i = 0; i < 4; ++i) { t[i] = swap16_sum(v[i], v[i + 4]); t[i] += dpp<ROR4>(t[i]); t[i] += dpp<ROR8>(t[i]); }
  const float t0 = t[0], t1 = t[1], t2 = t[2], t3 = t[3];
  const float u0 = (lane & 4) ? t1 : t0, u1 = (lane & 4) ? t3 : t2;
  return (lane & 8) ? u1 : u0;
}

__global__ void __launch_bounds__(NT) fused_decode(Params p) {
  __shared__ __attribute__((aligned(16))) unsigned char smem[L_END];
  LAS unsigned char* L = (LAS unsigned char*)smem;
  const int tid = threadIdx.x, lane = tid & 63, w = __builtin_amdgcn_readfirstlane(tid >> 6);
  const int bid = blockIdx.x, xg = bid & 7, rk = bid >> 3;
  const int h = xg * 4 + (rk >> 3), b = (rk >> 2) & 1, sp = rk & 3;
  const int prod = b * 128 + h * 4 + sp;
  const int r = lane >> 4, c = lane & 15;
  const unsigned tstart = now32();
  u64 pc0;
  asm volatile("s_getpc_b64 %0" : "=s"(pc0));
  const __amdgpu_buffer_rsrc_t rsW = __builtin_amdgcn_make_buffer_rsrc((void*)p.ws, 0, (int)WS_END, 0x00020000);
  volatile LAS unsigned* ct = (volatile LAS unsigned*)(L + L_CT);
  STAMP(0);
  if (tid < 3) ct[tid] = 0u;
  __syncthreads();
  __builtin_amdgcn_sched_barrier(0);
  if (__builtin_expect(w < NAW, AUX_FIRST)) {
    __builtin_amdgcn_s_setprio(3);
    const int rg = lane >> 2, c4 = lane & 3, gcol = (lane & 32) + 8 * c4 + 4 * (r & 1) + (rg & 3);
    const unsigned e0 = __hip_atomic_load((GAS unsigned*)(p.ws + WS_E), RLX_AGENT);
    const u32x4 x0 = *(const GAS u32x4*)(p.x + lane * 8), x1 = *(const GAS u32x4*)(p.x + DM + lane * 8);
    const unsigned xa = p.x[256 * b + 64 * w + lane], xb = p.x[DM + 256 * b + 64 * w + lane];
    u32x4 wv[4];
    {
      const GAS u16* src = (const GAS u16*)p.wq + (size_t)(256 * b + 64 * w + 4 * rg) * (NH * DH) + h * DH + sp * 32 + c4 * 8;
#pragma unroll
      for (int i = 0; i < 4; ++i) wv[i] = W_NT ? __builtin_nontemporal_load((const GAS u32x4*)(src + (size_t)i * (NH * DH))) : *(const GAS u32x4*)(src + (size_t)i * (NH * DH));
    }
    unsigned cpf = 0;
    if (CODE_PF && w == 3) { const int li = rk * CPL + lane; if (lane < CPL && li < CODE_LINES) cpf = *(const GAS unsigned*)((pc0 & ~127ull) + (u64)li * 128); }
    __builtin_amdgcn_sched_barrier(0);
    const unsigned tag = e0 + 1u;
    float m0, d0, m1, d1;
    ln_stats(x0, m0, d0); ln_stats(x1, m1, d1);
    {
      LAS float* pq = (LAS float*)(L + L_PQ);
      pq[w * 64 + gcol] = gemv64x32(wv, ln_apply(bf_lo(xa), m0, d0), ln_apply(bf_lo(xb), m1, d1), lane);
      if (lds_arrive_last(L, 0)) {
        const int l2 = 2 * (lane & 31);
        const f32x2 s = (*(LAS f32x2*)(pq + l2) + *(LAS f32x2*)(pq + 64 + l2)) + (*(LAS f32x2*)(pq + 128 + l2) + *(LAS f32x2*)(pq + 192 + l2));
        st_gran2(rsW, lane < 32 ? (int)WS_QP + (((h * 4 + sp) * 2 + b) * 64 + l2) * 8 : OOB, tag, s[0], s[1]);
        STAMP(1);
        const int qo = (int)WS_QP + (((h * 4 + r) * 2) * 64 + b * 32 + 2 * c) * 8;
        u32x4 g0, g1;
        __builtin_amdgcn_s_sleep(Q_WAIT);
        for (unsigned spins = 0; spins < SPIN_CAP; ++spins) {
          asm volatile("" ::: "memory");
          g0 = ld_gran2(rsW, qo); g1 = ld_gran2(rsW, qo + 512);
          if (__all(g0[1] == tag && g0[3] == tag && g1[1] == tag && g1[3] == tag)) break;
        }
        ((LAS unsigned*)(L + L_Q))[lane] = pkbf(__uint_as_float(g0[0]) + __uint_as_float(g1[0]), __uint_as_float(g0[2]) + __uint_as_float(g1[2]));
        if (lane == 0) { ct[3] = tag; __hip_atomic_store((LAS unsigned*)(L + L_CT) + 2, 1u, __ATOMIC_RELEASE, __HIP_MEMORY_SCOPE_WORKGROUP); }
        STAMP(2);
      }
    }
    asm volatile("" :: "v"(cpf));
    __builtin_amdgcn_s_setprio(2);
    if (!NOFFN && rk < 22 && (xg & 1) * 22 + rk < NSL) {
      const int j = (xg & 1) * 22 + rk, mat = (xg >> 1) & 1, hf = xg >> 2;
      wait_until(tstart, WO_T0 - 200);
      const unsigned ya = p.x[256 * hf + 64 * w + lane], yb = p.x[DM + 256 * hf + 64 * w + lane];
      const GAS u16* src = (const GAS u16*)(mat ? p.wu : p.wg) + (size_t)(256 * hf + 64 * w + 4 * rg) * FF + j * 32 + c4 * 8;
#pragma unroll
      for (int i = 0; i < 4; ++i) wv[i] = W_NT ? __builtin_nontemporal_load((const GAS u32x4*)(src + (size_t)i * FF)) : *(const GAS u32x4*)(src + (size_t)i * FF);
      LAS float* pf = (LAS float*)(L + L_PF);
      pf[w * 64 + gcol] = gemv64x32(wv, ln_apply(bf_lo(ya), m0, d0), ln_apply(bf_lo(yb), m1, d1), lane);
      if (lds_arrive_last(L, 1)) {
        const int l2 = 2 * (lane & 31);
        const f32x2 s = (*(LAS f32x2*)(pf + l2) + *(LAS f32x2*)(pf + 64 + l2)) + (*(LAS f32x2*)(pf + 128 + l2) + *(LAS f32x2*)(pf + 192 + l2));
        st_gran2(rsW, lane < 32 ? (int)WS_GU + (((j * 2 + mat) * 2 + hf) * 64 + l2) * 8 : OOB, tag, s[0], s[1]);
      }
    }
    STAMP(3);
    u32x4 bf[4][8];
    {
      const GAS u16* src = (const GAS u16*)p.wo + (size_t)(h * DH + 8 * r) * DM + w * 128 + c * 8;
#pragma unroll
      for (int s = 0; s < 4; ++s) {
        if (!NOFFN && s == 1 && rk >= 22 && rk < 22 + NZG) {
          const int g = rk - 22, u0 = 160 * g + 40 * w, unit = u0 + lane;
          const bool live = lane < 40 && unit < FF;
          const __amdgpu_buffer_rsrc_t rsD = __builtin_amdgcn_make_buffer_rsrc((void*)p.wd, 0, FF * DM * 2, 0x00020000);
          u32x4 dv[5];
#pragma unroll
          for (int i = 0; i < 5; ++i) dv[i] = __builtin_amdgcn_raw_buffer_load_b128(rsD, ((u0 + 8 * i + (lane >> 3)) * DM + 64 * xg + (lane & 7) * 8) * 2, 0, 2);
          const int go = live ? (int)WS_GU + ((unit >> 5) * 256 + (unit & 31)) * 8 : OOB;
          u32x2 gv[8];
          for (unsigned spins = 0; spins < SPIN_CAP; ++spins) {
            asm volatile("" ::: "memory");
#pragma unroll
            for (int i = 0; i < 8; ++i) gv[i] = __builtin_amdgcn_raw_buffer_load_b64(rsW, go, i * 256, 16);
            bool ok = true;
#pragma unroll
            for (int i = 0; i < 8; ++i) ok &= gv[i][1] == tag;
            if (__all(ok || !live)) break;
            __builtin_amdgcn_s_sleep(32);
          }
          LAS float* hb = (LAS float*)(L + L_H) + w * 128;
#pragma unroll
          for (int bb = 0; bb < 2; ++bb) {
            const float gs = rbf(__uint_as_float(gv[bb][0]) + __uint_as_float(gv[2 + bb][0])), us = rbf(__uint_as_float(gv[4 + bb][0]) + __uint_as_float(gv[6 + bb][0]));
            hb[bb * 64 + lane] = live ? rbf(rbf(gelu_tanh(gs)) * us) : 0.f;
          }
          float z0[8], z1[8];
#pragma unroll
          for (int i = 0; i < 8; ++i) z0[i] = z1[i] = 0.f;
#pragma unroll
          for (int i = 0; i < 5; ++i) {
            const float a0 = hb[8 * i + (lane >> 3)], a1 = hb[64 + 8 * i + (lane >> 3)];
#pragma unroll
            for (int k = 0; k < 4; ++k) {
              const float lo = bf_lo(dv[i][k]), hi = bf_hi(dv[i][k]);
              z0[2 * k] = fmaf(a0, lo, z0[2 * k]); z0[2 * k + 1] = fmaf(a0, hi, z0[2 * k + 1]);
              z1[2 * k] = fmaf(a1, lo, z1[2 * k]); z1[2 * k + 1] = fmaf(a1, hi, z1[2 * k + 1]);
            }
          }
          float v[8], t[4];
#pragma unroll
          for (int i = 0; i < 8; ++i) v[i] = swap32_sum(z0[i], z1[i]);
#pragma unroll
          for (int i = 0; i < 4; ++i) { t[i] = swap16_sum(v[i], v[i + 4]); t[i] += dpp<ROR8>(t[i]); }
          const float t0 = t[0], t1 = t[1], t2 = t[2], t3 = t[3];
          LAS float* pf = (LAS float*)(L + L_PF);
          *(LAS f32x2*)(pf + w * 128 + (lane >> 5) * 64 + (lane & 7) * 8 + 4 * (r & 1) + ((lane & 8) >> 2)) = f32x2{(lane & 8) ? t2 : t0, (lane & 8) ? t3 : t1};
          if (lds_arrive_last(L, 1)) {
            const int l2 = 2 * lane;
            const f32x2 sm = (*(LAS f32x2*)(pf + l2) + *(LAS f32x2*)(pf + 128 + l2)) + (*(LAS f32x2*)(pf + 256 + l2) + *(LAS f32x2*)(pf + 384 + l2));
            st_gran2(rsW, (int)WS_Z + ((g * 8 + xg) * 128 + l2) * 8, tag, sm[0], sm[1]);
          }
        }
        if (!NOFFN && s == 3 && w == 2 && sp == 0) {
          const int zo = (int)WS_Z + ((r * 8 + (h >> 2)) * 128 + b * 64 + (h & 3) * 16 + c) * 8;
          const float xres = bf_lo(p.x[b * DM + h * 16 + c]);
          u32x2 zv[3];
          bool good = false;
          for (unsigned spins = 0; spins < SPIN_CAP; ++spins) {
            asm volatile("" ::: "memory");
            zv[0] = ld_gran(rsW, zo); zv[1] = ld_gran(rsW, zo + 4 * 8192); zv[2] = ld_gran(rsW, r == 0 ? zo + 8 * 8192 : OOB);
            if (__all(zv[0][1] == tag && zv[1][1] == tag && (r != 0 || zv[2][1] == tag))) { good = true; break; }
            __builtin_amdgcn_s_sleep(16);
          }
          const float z = rbf(xrow_sum((__uint_as_float(zv[0][0]) + __uint_as_float(zv[1][0])) + __uint_as_float(zv[2][0])));
          float g = rbf(z / (1.0f + __expf(-z)));
          if (!good) g = __uint_as_float(0x7fc00000u);
          if (lane < 16) { ((LAS float*)(L + L_G))[lane] = g; ((LAS float*)(L + L_G))[16 + lane] = xres; }
        }
        if (CODE_PF > 1 && s == 3 && w == 3) { const int li = rk * CPL + lane; unsigned c2 = 0; if (lane < CPL && li < CODE_LINES) c2 = *(const volatile GAS unsigned*)((pc0 & ~127ull) + (u64)li * 128); asm volatile("" :: "v"(c2)); }
        u32x4 t[8];
#pragma unroll
        for (int j = 0; j < 8; ++j) {
          wait_until(tstart, WO_T0 + (8 * s + j) * WO_DT);
          __builtin_amdgcn_sched_barrier(0);
          t[j] = WO_MODE == 2 ? u32x4{1u, 2u, 3u, (unsigned)lane} : WO_MODE == 1 ? __builtin_nontemporal_load((const GAS u32x4*)(src + (32 * s + j) * DM)) : *(const GAS u32x4*)(src + (32 * s + j) * DM);
          __builtin_amdgcn_sched_barrier(0);
        }
#pragma unroll
        for (int e = 0; e < 8; ++e)
#pragma unroll
          for (int k = 0; k < 4; ++k)
            bf[s][e][k] = __builtin_amdgcn_perm(t[2 * k + 1][e >> 1], t[2 * k][e >> 1], (e & 1) ? 0x07060302u : 0x05040100u);
#pragma unroll
        for (int e = 0; e < 8; ++e) asm volatile("" : "+v"(bf[s][e]));
        __builtin_amdgcn_sched_barrier(0);
      }
    }
    STAMP(5);
    __syncthreads();
    STAMP(6);
    {
      u32x4 af[4];
#pragma unroll
      for (int s = 0; s < 4; ++s) af[s] = *(LAS u32x4*)(L + L_REC + (c & 7) * REC_STRIDE + (32 * s + 8 * r) * 2);
      const f32x4 mlr = *(LAS f32x4*)(L + L_MLW + (r & 1) * 16), mlo = *(LAS f32x4*)(L + L_MLW + ((r & 1) ^ 1) * 16);
      const float M = fmaxf(fmaxf(mlr[0], mlr[2]), fmaxf(mlo[0], mlo[2]));
      float fa = __builtin_amdgcn_exp2f(mlr[0] - M), fb = __builtin_amdgcn_exp2f(mlr[2] - M);
      const float Lh = fmaf(fa, mlr[1], fb * mlr[3]);
      const float Ls = swap16_sum(Lh, Lh);
      const float inv = __builtin_amdgcn_rcpf(Ls);
      fa *= inv; fb *= inv;
      const unsigned lse = __float_as_uint(M + __builtin_amdgcn_logf(Ls));
      float u[8];
#pragma unroll
      for (int e = 0; e < 8; ++e) {
        f32x4 d = {0.f, 0.f, 0.f, 0.f};
#pragma unroll
        for (int s = 0; s < 4; ++s) d = mfma16(af[s], bf[s][e], d);
        u[e] = fmaf(fa, d[0] + d[1], fb * (d[2] + d[3]));
      }
      float t[4];
#pragma unroll
      for (int i = 0; i < 4; ++i) { const float lo = u[i], hi = u[4 + i]; t[i] = (r & 2) ? hi : lo; }
      const float y0 = swap16_sum(t[0], t[2]), y1 = swap16_sum(t[1], t[3]);
      __builtin_amdgcn_raw_buffer_store_b128(u32x4{__float_as_uint(y0), (tag << 16) | (lse >> 16), __float_as_uint(y1), (tag << 16) | (lse & 0xffffu)}, rsW,
                                             (int)WS_Y + (prod * DM + w * 128 + c * 8 + 2 * r) * 8, 0, 16);
    }
    STAMP(7);
    STAMP_OUT();
    return;
  }
  const int ws = w - NAW;
  const size_t kvbase = ((size_t)(b * NH + h) * SK + (size_t)sp * KPS) * DH;
  const __amdgpu_buffer_rsrc_t rsK = __builtin_amdgcn_make_buffer_rsrc((void*)(p.K + kvbase), 0, KPS * DH * 2, 0x00020000);
  const __amdgpu_buffer_rsrc_t rsV = __builtin_amdgcn_make_buffer_rsrc((void*)(p.V + kvbase), 0, KPS * DH * 2, 0x00020000);
  const int voff = ws * 4096 + lane * 16;
#define LDK(so, u) __builtin_amdgcn_raw_buffer_load_b128(rsK, voff + (u) * 1024, (so), K_AUX)
#define LDV(so, u) __builtin_amdgcn_raw_buffer_load_b128(rsV, voff + (u) * 1024, (so), V_AUX)
  __builtin_amdgcn_s_setprio(1);
  u32x4 kk[3][4], vv[3][4];
#pragma unroll
  for (int d = 0; d < 2; ++d) {
#pragma unroll
    for (int u = 0; u < 4; ++u) {
      if (PRO_THROTTLE && d > 0) asm volatile("s_waitcnt vmcnt(%0)" :: "i"(PRO_THROTTLE) : "memory");
      kk[d][u] = LDK(d * STEPB, u);
      __builtin_amdgcn_sched_barrier(0);
    }
#pragma unroll
    for (int u = 0; u < 4; ++u) {
      if (PRO_THROTTLE && d > 0) asm volatile("s_waitcnt vmcnt(%0)" :: "i"(PRO_THROTTLE) : "memory");
      vv[d][u] = LDV(d * STEPB, u);
      __builtin_amdgcn_sched_barrier(0);
    }
  }
  const unsigned xbase = (unsigned)(size_t)(L + L_X) + ws * 8192;
#define K_SUF " nt"
#define V_SUF " nt"
#pragma unroll 1
  for (int x = 0; x < NX; ++x) {
    const unsigned dst = xbase + x * (NSW * 8192), so = (2 + x) * STEPB;
#define XDMA(RS, O, LO, SUF) asm volatile("s_waitcnt vmcnt(%4)\n\ts_add_u32 m0, %1, " #LO "\n\ts_nop 0\n\tbuffer_load_dwordx4 %0, %2, %3 offen offset:" #O SUF " lds" \
                                          :: "v"(voff), "s"(dst), "s"(RS), "s"(so), "i"(PRO_THROTTLE ? PRO_THROTTLE : 63) : "memory", "scc")
    XDMA(rsK, 0, 0, K_SUF); XDMA(rsK, 1024, 0, K_SUF); XDMA(rsK, 2048, 0, K_SUF); XDMA(rsK, 3072, 0, K_SUF);
    XDMA(rsV, 0, 4096, V_SUF); XDMA(rsV, 1024, 4096, V_SUF); XDMA(rsV, 2048, 4096, V_SUF); XDMA(rsV, 3072, 4096, V_SUF);
#undef XDMA
  }
  STAMP(6);
  while (__hip_atomic_load((LAS unsigned*)(L + L_CT) + 2, __ATOMIC_ACQUIRE, __HIP_MEMORY_SCOPE_WORKGROUP) == 0u) __builtin_amdgcn_s_sleep(1);
  const u32x4 qv = *(LAS u32x4*)(L + L_Q + c * 16);
  const unsigned tag = ct[3];
  STAMP(1);
  float m = -INFINITY, l = 0.f, acc[8];
#pragma unroll
  for (int i = 0; i < 8; ++i) acc[i] = 0.f;
  constexpr float SCALE = 0.08838834764831845f, LOG2E = 1.4426950408889634f;
#define PACE_WAIT(n2) do { if (PACE) { const unsigned tgt_ = ((unsigned)((n2) * PACE) >> 6) + PACE0; while (now32() - tstart < tgt_) __builtin_amdgcn_s_sleep(1); } } while (0)
#define STEP(d, REFILL, s)                                                                                \
  {                                                                                                       \
    float sc[4];                                                                                          \
    _Pragma("unroll") for (int u = 0; u < 4; ++u) {                                                       \
      float t = dot2(kk[d][u][0], qv[0], 0.f);                                                            \
      t = dot2(kk[d][u][1], qv[1], t); t = dot2(kk[d][u][2], qv[2], t); t = dot2(kk[d][u][3], qv[3], t);  \
      sc[u] = t;                                                                                          \
    }                                                                                                     \
    if (REFILL) {                                                                                         \
      _Pragma("unroll") for (int u = 0; u < 4; ++u) asm volatile("" : "+v"(sc[u]));                       \
      __builtin_amdgcn_sched_barrier(0);                                                                  \
      if ((s) < NSTEP) PACE_WAIT(2 * (s));                                                                \
      __builtin_amdgcn_sched_barrier(0);                                                                  \
      _Pragma("unroll") for (int u = 0; u < 4; ++u) kk[d][u] = LDK((s) * STEPB, u);                       \
      __builtin_amdgcn_sched_barrier(0);                                                                  \
    }                                                                                                     \
    _Pragma("unroll") for (int u = 0; u < 4; ++u) sc[u] = rbf(rbf(row_sum(sc[u])) * SCALE) * LOG2E;       \
    const float mx = fmaxf(fmaxf(m, fmaxf(sc[0], sc[1])), fmaxf(sc[2], sc[3]));                           \
    const float al = __builtin_amdgcn_exp2f(m - mx);                                                      \
    m = mx;                                                                                               \
    float pp[4];                                                                                          \
    _Pragma("unroll") for (int u = 0; u < 4; ++u) pp[u] = __builtin_amdgcn_exp2f(sc[u] - mx);             \
    l = fmaf(l, al, (pp[0] + pp[1]) + (pp[2] + pp[3]));                                                   \
    _Pragma("unroll") for (int i = 0; i < 8; ++i) acc[i] *= al;                                           \
    _Pragma("unroll") for (int u = 0; u < 4; ++u)                                                         \
      _Pragma("unroll") for (int k = 0; k < 4; ++k) {                                                     \
        acc[2 * k] = fmaf(pp[u], bf_lo(vv[d][u][k]), acc[2 * k]);                                         \
        acc[2 * k + 1] = fmaf(pp[u], bf_hi(vv[d][u][k]), acc[2 * k + 1]);                                 \
      }                                                                                                   \
    _Pragma("unroll") for (int i = 0; i < 8; ++i) asm volatile("" : "+v"(acc[i]));                        \
    __builtin_amdgcn_sched_barrier(0);                                                                    \
    if (REFILL) {                                                                                         \
      if ((s) < NSTEP) PACE_WAIT(2 * (s) + 1);                                                            \
      __builtin_amdgcn_sched_barrier(0);                                                                  \
      _Pragma("unroll") for (int u = 0; u < 4; ++u) vv[d][u] = LDV((s) * STEPB, u);                       \
      __builtin_amdgcn_sched_barrier(0);                                                                  \
    }                                                                                                     \
  }
#pragma unroll 1
  for (int s = 2 + NX; s < NSTEP + 2; s += 2) {
    STEP(0, true, s)
    STEP(1, true, s + 1)
    const int x = (s - 2 - NX) >> 1;
    if (x < NX) {
      asm volatile("s_waitcnt vmcnt(16)" ::: "memory");
      const LAS unsigned char* xp = L + L_X + ws * 8192 + x * (NSW * 8192) + lane * 16;
#pragma unroll
      for (int u = 0; u < 4; ++u) { kk[2][u] = *(const LAS u32x4*)(xp + u * 1024); vv[2][u] = *(const LAS u32x4*)(xp + 4096 + u * 1024); }
      STEP(2, false, 0)
    }
  }
#undef STEP
  STAMP(2);
  {
    const float M = xrow_max(m);
    const float f = __builtin_amdgcn_exp2f(m - M);
    l = xrow_sum(l * f);
    float t[4];
#pragma unroll
    for (int i = 0; i < 4; ++i) t[i] = swap32_sum(acc[i] * f, acc[i + 4] * f);
    const float c0 = swap16_sum(t[0], t[2]), c1 = swap16_sum(t[1], t[3]);
    const unsigned hi = pkbf(c0, c1);
    const unsigned lo = pkbf(c0 - bf_lo(hi), c1 - bf_hi(hi));
    *(LAS unsigned*)(L + L_REC + (2 * ws) * REC_STRIDE + (8 * c + 2 * r) * 2) = hi;
    *(LAS unsigned*)(L + L_REC + (2 * ws + 1) * REC_STRIDE + (8 * c + 2 * r) * 2) = lo;
    if (lane == 0) { ((LAS float*)(L + L_MLW))[2 * ws] = M; ((LAS float*)(L + L_MLW))[2 * ws + 1] = l; }
  }
  STAMP(3);
  __syncthreads();
  STAMP(7);
  if (sp != 0) { STAMP_OUT(); return; }
  {
    const int yo = (int)WS_Y + ((b * 128 + (8 * ws + (lane >> 5)) * 4 + (lane & 3)) * DM + 16 * h + 2 * ((lane >> 2) & 7)) * 8;
    const unsigned t16 = tag & 0xffffu;
    u32x4 yv[4];
    bool okv[4] = {false, false, false, false}, good = false;
    yv[0] = yv[1] = yv[2] = yv[3] = u32x4{0u, 0u, 0u, 0u};
    __builtin_amdgcn_s_sleep(C_WAIT);
    for (unsigned spins = 0; spins < SPIN_CAP; ++spins) {
      u32x4 ty[4];
      asm volatile("" ::: "memory");
#pragma unroll
      for (int i = 0; i < 4; ++i) ty[i] = __builtin_amdgcn_raw_buffer_load_b128(rsW, yo, okv[i] ? 0x40000000 : i * (8 * DM * 8), 16);
      asm volatile("" ::: "memory");
#pragma unroll
      for (int i = 0; i < 4; ++i) { if (!okv[i]) { yv[i] = ty[i]; okv[i] = __all((ty[i][1] >> 16) == t16 && (ty[i][3] >> 16) == t16); } }
      if (okv[0] && okv[1] && okv[2] && okv[3]) { good = true; break; }
      __builtin_amdgcn_s_sleep(1);
    }
    STAMP(4);
    float s0 = 0.f, s1 = 0.f;
#pragma unroll
    for (int i = 0; i < 4; ++i) {
      const float lse = __uint_as_float((yv[i][1] << 16) | (yv[i][3] & 0xffffu));
      float M = fmaxf(lse, dpp<XOR1>(lse)); M = fmaxf(M, dpp<XOR2>(M));
      const float e = __builtin_amdgcn_exp2f(lse - M);
      float S = e + dpp<XOR1>(e); S += dpp<XOR2>(S);
      const float wt = e * __builtin_amdgcn_rcpf(S);
      s0 = fmaf(wt, __uint_as_float(yv[i][0]), s0); s1 = fmaf(wt, __uint_as_float(yv[i][2]), s1);
    }
    float t = swap32_sum(s0, s1);
    t += dpp<XOR1>(t); t += dpp<XOR2>(t);
    if (!good) t = __uint_as_float(0x7fc00000u);
    LAS float* red = (LAS float*)(L + L_RED);
    if ((lane & 3) == 0) red[ws * 16 + 2 * ((lane >> 2) & 7) + (lane >> 5)] = t;
    __syncthreads();
    if (ws == 0) {
      const float sv = rbf(xrow_sum(red[lane]));
      const float g = ((LAS float*)(L + L_G))[c], xres = ((LAS float*)(L + L_G))[16 + c];
      if (lane < 16) p.out[b * DM + h * 16 + c] = (u16)bfbits(xres + rbf(sv * g));
      if (prod == 0 && lane == 0) __hip_atomic_store((GAS unsigned*)(p.ws + WS_E), tag, RLX_AGENT);
    }
    STAMP(5);
    STAMP_OUT();
  }
}

extern "C" void kernel_launch(void* const* d_in, const int* in_sizes, int n_in, void* d_out, int out_size, void* d_ws, size_t ws_size,
                              hipStream_t stream) {
  Params p;
  p.x = (const u16*)d_in[0]; p.K = (const u16*)d_in[1]; p.V = (const u16*)d_in[2]; p.wq = (const u16*)d_in[3]; p.wo = (const u16*)d_in[4];
  p.wg = (const u16*)d_in[6]; p.wu = (const u16*)d_in[7]; p.wd = (const u16*)d_in[8];
  p.out = (u16*)d_out; p.ws = (unsigned char*)d_ws;
  fused_decode<<<NWG, NT, 0, stream>>>(p);
}
```

```cpp
#include <hip/hip_runtime.h>
#include <hip/hip_bf16.h>
#include <stdint.h>

typedef unsigned u32x4 __attribute__((ext_vector_type(4)));
typedef unsigned u32x2 __attribute__((ext_vector_type(2)));
typedef float f32x4 __attribute__((ext_vector_type(4)));
typedef float f32x2 __attribute__((ext_vector_type(2)));
typedef __bf16 bf16x2 __attribute__((ext_vector_type(2)));
typedef __bf16 bf16x8 __attribute__((ext_vector_type(8)));
typedef unsigned short u16;
typedef unsigned long long u64;
#define GAS __attribute__((address_space(1)))
#define LAS __attribute__((address_space(3)))
#define RLX_AGENT __ATOMIC_RELAXED, __HIP_MEMORY_SCOPE_AGENT

#define PACE 3700
#define WIN 20
#define WQ 30
#define RPU 4
#define SYNC_OFF 135
#define C_OFF 225
#define CODE_LINES 140
constexpr int WO_T0 = 900, WO_DT = 80, Q_WAIT = 32, SYNC_T0 = 800, SYNC_T = 400, SYNC_D = 150, SYNC_STOP = 14, C_MIN = 56, C_MAX = 130;

constexpr int DM = 512, NH = 32, SK = 8192, DH = 128, FF = 1376;
constexpr int NWG = 256, NAW = 4, NSW = 4, NT = (NAW + NSW + 2) * 64;
constexpr int NSPLIT = 4, KPS = SK / NSPLIT, NSTEP = KPS / (16 * NSW), NBLK = KPS / 16, NSLOT = 19;
constexpr int NSL = FF / 32, NZG = 8;
constexpr size_t WS_E = 0, WS_QP = 4096, WS_GU = WS_QP + (size_t)NH * 8 * 64 * 8, WS_Z = WS_GU + (size_t)45 * 4 * 64 * 8,
                 WS_Y = WS_Z + (size_t)NZG * 8 * 128 * 8, WS_PT = WS_Y + (size_t)NWG * DM * 8, WS_END = WS_PT + 32 * 4352;
constexpr int REC_STRIDE = 272;
constexpr int L_Q = 0, L_REC = 256, L_MLW = L_REC + 8 * REC_STRIDE, L_G = L_MLW + 32, L_CT = L_G + 128, L_RED = L_CT + 48, L_PQ = L_RED + NSW * 64,
              L_PF = L_PQ + NAW * 256, L_H = L_PF + NAW * 512, L_X = 8192, L_END = L_X + NSLOT * 8192;
static_assert(L_H + NAW * 512 <= L_X, "");
constexpr unsigned SPIN_CAP = 1u << 18;
constexpr int OOB = 0x7ffffff0;

struct Params {
  const u16 *x, *K, *V, *wq;
  unsigned char* ws;
  const u16 *wo, *wg, *wu, *wd;
  u16* out;
};

__device__ __forceinline__ float bf_lo(unsigned u) { return __builtin_bit_cast(float, u << 16); }
__device__ __forceinline__ float bf_hi(unsigned u) { return __builtin_bit_cast(float, u & 0xffff0000u); }
__device__ __forceinline__ float rbf(float f) { return (float)(__bf16)f; }
__device__ __forceinline__ unsigned bfbits(float f) { __bf16 b = (__bf16)f; return (unsigned)__builtin_bit_cast(u16, b); }
__device__ __forceinline__ unsigned pkbf(float lo, float hi) { const bf16x2 v = {(__bf16)lo, (__bf16)hi}; return __builtin_bit_cast(unsigned, v); }
__device__ __forceinline__ float dot2(unsigned k, unsigned q, float acc) {
  return __builtin_amdgcn_fdot2_f32_bf16(__builtin_bit_cast(bf16x2, k), __builtin_bit_cast(bf16x2, q), acc, false);
}
template <int DC> __device__ __forceinline__ float dpp(float x) {
  return __builtin_bit_cast(float, __builtin_amdgcn_mov_dpp(__builtin_bit_cast(int, x), DC, 0xf, 0xf, true));
}
__device__ __forceinline__ float quad_max(float x) {
  float t, u;
  asm("s_nop 1\n\tv_max_f32_dpp %0, %2, %2 quad_perm:[1,0,3,2] row_mask:0xf bank_mask:0xf\n\ts_nop 1\n\tv_max_f32_dpp %1, %0, %0 quad_perm:[2,3,0,1] row_mask:0xf bank_mask:0xf"
      : "=&v"(t), "=v"(u) : "v"(x));
  return u;
}
constexpr int XOR1 = 0xB1, XOR2 = 0x4E, XOR7 = 0x141, ROR8 = 0x128, ROR4 = 0x124;
__device__ __forceinline__ float swap16_sum(float a, float b) {
  auto s = __builtin_amdgcn_permlane16_swap(__float_as_uint(a), __float_as_uint(b), false, false);
  return __uint_as_float(s[0]) + __uint_as_float(s[1]);
}
__device__ __forceinline__ float swap32_sum(float a, float b) {
  auto s = __builtin_amdgcn_permlane32_swap(__float_as_uint(a), __float_as_uint(b), false, false);
  return __uint_as_float(s[0]) + __uint_as_float(s[1]);
}
__device__ __forceinline__ float xrow_sum(float x) { x = swap16_sum(x, x); return swap32_sum(x, x); }
__device__ __forceinline__ float xrow_max(float x) {
  auto s = __builtin_amdgcn_permlane16_swap(__float_as_uint(x), __float_as_uint(x), false, false);
  x = fmaxf(__uint_as_float(s[0]), __uint_as_float(s[1]));
  auto t = __builtin_amdgcn_permlane32_swap(__float_as_uint(x), __float_as_uint(x), false, false);
  return fmaxf(__uint_as_float(t[0]), __uint_as_float(t[1]));
}
__device__ __forceinline__ float row_sum(float d) {
  d += dpp<XOR1>(d); d += dpp<XOR2>(d); d += dpp<XOR7>(d); d += dpp<ROR8>(d);
  return d;
}
__device__ __forceinline__ float wave_sum(float d) { return xrow_sum(row_sum(d)); }
__device__ __forceinline__ void st_gran2(__amdgpu_buffer_rsrc_t rs, int off, unsigned tag, float a, float b) {
  __builtin_amdgcn_raw_buffer_store_b128(u32x4{__float_as_uint(a), tag, __float_as_uint(b), tag}, rs, off, 0, 16);
}
__device__ __forceinline__ u32x4 ld_gran2(__amdgpu_buffer_rsrc_t rs, int off) { return __builtin_amdgcn_raw_buffer_load_b128(rs, off, 0, 16); }
__device__ __forceinline__ u32x2 ld_gran(__amdgpu_buffer_rsrc_t rs, int off) { return __builtin_amdgcn_raw_buffer_load_b64(rs, off, 0, 16); }
__device__ __forceinline__ f32x4 mfma16(u32x4 a, u32x4 b, f32x4 c) {
  return __builtin_amdgcn_mfma_f32_16x16x32_bf16(__builtin_bit_cast(bf16x8, a), __builtin_bit_cast(bf16x8, b), c, 0, 0, 0);
}
__device__ __forceinline__ unsigned now32() { return (unsigned)__builtin_amdgcn_s_memrealtime(); }
__device__ __forceinline__ void wait_until(unsigned t0, unsigned due) { while (now32() - t0 < due) __builtin_amdgcn_s_sleep(2); }
__device__ __forceinline__ bool lds_arrive_last(LAS unsigned char* L, int idx) {
  unsigned old = 0;
  if ((threadIdx.x & 63) == 0) old = __hip_atomic_fetch_add((LAS unsigned*)(L + L_CT) + idx, 1u, __ATOMIC_ACQ_REL, __HIP_MEMORY_SCOPE_WORKGROUP);
  return __builtin_amdgcn_readfirstlane(old) == NAW - 1;
}

__device__ __forceinline__ void ln_stats(u32x4 v, float& mb, float& den) {
  float f[8];
#pragma unroll
  for (int i = 0; i < 4; ++i) { f[2 * i] = bf_lo(v[i]); f[2 * i + 1] = bf_hi(v[i]); }
  float s = ((f[0] + f[1]) + (f[2] + f[3])) + ((f[4] + f[5]) + (f[6] + f[7]));
  s = wave_sum(s);
  const float mean = s * (1.0f / DM);
  float q = 0.f;
#pragma unroll
  for (int i = 0; i < 8; ++i) { const float d = f[i] - mean; q = fmaf(d, d, q); }
  q = wave_sum(q);
  const float var = rbf(q * (1.0f / DM));
  mb = rbf(mean);
  den = rbf(sqrtf(rbf(var + 1e-5f)));
}
__device__ __forceinline__ float ln_apply(float x, float mb, float den) { return rbf(rbf(x - mb) / den); }
__device__ __forceinline__ float gelu_tanh(float x) {
  const float inner = 0.7978845608028654f * (x + 0.044715f * x * x * x);
  return 0.5f * x * (1.0f + tanhf(inner));
}
__device__ __forceinline__ float gemv64x32(const u32x4 (&wv)[4], float n0, float n1, int lane) {
  const float s0[4] = {dpp<0x00>(n0), dpp<0x55>(n0), dpp<0xAA>(n0), dpp<0xFF>(n0)};
  const float s1[4] = {dpp<0x00>(n1), dpp<0x55>(n1), dpp<0xAA>(n1), dpp<0xFF>(n1)};
  float a0[8], a1[8];
#pragma unroll
  for (int i = 0; i < 8; ++i) a0[i] = a1[i] = 0.f;
#pragma unroll
  for (int i = 0; i < 4; ++i)
#pragma unroll
    for (int k = 0; k < 4; ++k) {
      const float lo = bf_lo(wv[i][k]), hi = bf_hi(wv[i][k]);
      a0[2 * k] = fmaf(s0[i], lo, a0[2 * k]); a0[2 * k + 1] = fmaf(s0[i], hi, a0[2 * k + 1]);
      a1[2 * k] = fmaf(s1[i], lo, a1[2 * k]); a1[2 * k + 1] = fmaf(s1[i], hi, a1[2 * k + 1]);
    }
  float v[8], t[4];
#pragma unroll
  for (int i = 0; i < 8; ++i) v[i] = swap32_sum(a0[i], a1[i]);
#pragma unroll
  for (int i = 0; i < 4; ++i) { t[i] = swap16_sum(v[i], v[i + 4]); t[i] += dpp<ROR4>(t[i]); t[i] += dpp<ROR8>(t[i]); }
  const float t0 = t[0], t1 = t[1], t2 = t[2], t3 = t[3];
  const float u0 = (lane & 4) ? t1 : t0, u1 = (lane & 4) ? t3 : t2;
  return (lane & 8) ? u1 : u0;
}

__device__ __forceinline__ void loader_wave(const void* kb, const void* vb, int voff, unsigned tstart) {
#define LD1(O, N, R) "s_waitcnt vmcnt(" #N ")\n\tbuffer_load_dwordx4 " R ", s[64:67], s88 offen offset:" #O " nt lds\n\t"
#define LD4(N) LD1(0, N, "%2") LD1(1024, N, "%14") LD1(2048, N, "%15") LD1(3072, N, "%16")
#define VAR(N) LD4(N) "s_setpc_b64 s[80:81]\n\t"
#define DRN(N) "s_waitcnt vmcnt(" #N ")\n\tv_add_u32 v5, 1, v5\n\tds_write_b32 v2, v5 offset:%c4+32\n\t"
  asm volatile(
      "s_setprio 3\n\t"
      "s_mov_b32 s66, %c5\n\ts_mov_b32 s67, 0x20000\n\t"
      "s_getpc_b64 s[76:77]\n"
      ".Lld_pc:\n\t"
      "s_add_u32 s76, s76, .Lld_tab-.Lld_pc\n\ts_addc_u32 s77, s77, 0\n\t"
      "s_add_u32 s78, s76, 52*(%c11+1)\n\ts_addc_u32 s79, s77, 0\n\t"
      "s_mov_b32 s68, 0\n\ts_movk_i32 s69, %c6\n\ts_mov_b32 s70, %c7-4096\n\ts_mov_b32 s86, 0\n\ts_mov_b32 s87, 0x40000000\n\ts_movk_i32 s71, 0x1000\n\t"
      "v_mov_b32 v2, 0\n\tv_mov_b32 v4, %17\n\tds_write_b32 v2, v4 offset:%c4+36\n\tv_mov_b32 v5, -8\n\ts_mov_b32 s82, %c3\n\ts_branch .Lld_loop\n"
      ".Lld_outer:\n\t"
      "v_min_i32 v8, v8, v9\n\tv_min3_i32 v8, v8, v10, v11\n\tv_readfirstlane_b32 s85, v4\n\t"
      "s_add_i32 s83, s68, %c13-1-%c8\n\tv_readfirstlane_b32 s84, v8\n\ts_ashr_i32 s83, s83, 2\n\t"
      "s_cmp_gt_i32 s84, s83\n\ts_cbranch_scc0 .Lld_slow\n"
      ".Lld_go:\n\t"
      "s_sub_u32 s84, s74, s85\n\ts_sub_i32 s84, s84, s72\n\t"
      "s_mulk_i32 s84, %c10\n\ts_ashr_i32 s84, s84, 16\n\ts_add_i32 s84, s84, %c11\n\ts_max_i32 s84, s84, 0\n\ts_min_i32 s84, s84, 9\n\t"
      "s_mul_i32 s85, s84, 52\n\ts_add_u32 s78, s76, s85\n\ts_addc_u32 s79, s77, 0\n\t"
      "s_add_i32 s84, s84, 7\n\ts_lshr_b32 s84, s84, 1\n\ts_lshl_b32 s85, s68, 1\n\ts_sub_i32 s85, s85, s84\n\tv_mov_b32 v5, s85\n\t"
      "s_add_i32 s82, s68, %c13\n\ts_min_i32 s82, s82, %c12\n"
      ".Lld_loop:\n\t"
      "s_mov_b64 s[64:65], %0\n\ts_mov_b32 s88, s86\n\ts_mov_b32 m0, s69\n\t"
      "s_swappc_b64 s[80:81], s[78:79]\n\t"
      "s_mov_b64 s[64:65], %1\n\ts_mov_b32 s88, s87\n\ts_mov_b32 m0, s70\n\t"
      "s_add_i32 s68, s68, 1\n\ts_cmp_lg_u32 s68, s82\n\ts_cbranch_scc1 .Lld_nord\n\t"
      "ds_read_b128 v[8:11], v2 offset:%c4+16\n\tds_read_b32 v4, v2 offset:%c4+36\n\ts_memrealtime s[74:75]\n\t"
      "s_mul_i32 s72, s68, %c9\n\ts_add_i32 s72, s72, -%c9/2\n\ts_ashr_i32 s72, s72, 7\n"
      ".Lld_nord:\n\t"
      "s_swappc_b64 s[80:81], s[78:79]\n\t"
      "v_add_u32 v5, 2, v5\n\ts_waitcnt lgkmcnt(0)\n\tds_write_b32 v2, v5 offset:%c4+32\n\t"
      "s_mov_b32 s87, s86\n\ts_lshl_b32 s86, s68, 14\n\ts_add_u32 s70, s69, s71\n\ts_add_u32 s69, s70, s71\n\ts_cmp_eq_u32 s69, %c7\n\ts_cselect_b32 s69, %c6, s69\n\t"
      "s_cmp_lg_u32 s68, s82\n\ts_cbranch_scc1 .Lld_loop\n\t"
      "s_cmpk_lt_i32 s68, %c12\n\ts_cbranch_scc1 .Lld_outer\n\t"
      "s_branch .Lld_end\n"
      ".Lld_tab:\n\t"
      VAR(11) VAR(13) VAR(15) VAR(17) VAR(19) VAR(21) VAR(23) VAR(25) VAR(27) VAR(29)
      ".Lld_slow:\n\t"
      "ds_read_b128 v[8:11], v2 offset:%c4+16\n\ts_waitcnt lgkmcnt(0)\n\tv_min_i32 v8, v8, v9\n\tv_min3_i32 v8, v8, v10, v11\n\ts_nop 0\n\tv_readfirstlane_b32 s84, v8\n\t"
      "s_cmp_gt_i32 s84, s83\n\ts_cbranch_scc1 .Lld_go\n\t"
      "s_waitcnt vmcnt(0)\n\ts_lshl_b32 s84, s68, 1\n\tv_mov_b32 v5, s84\n\tds_write_b32 v2, v5 offset:%c4+32\n\t"
      "s_sleep 1\n\ts_branch .Lld_slow\n"
      ".Lld_end:\n\t"
      "s_mov_b32 s88, s87\n\ts_mov_b32 m0, s70\n\tv_mov_b32 v5, 2*%c12-9\n\t"
      LD4(40)
      DRN(36) DRN(32) DRN(28) DRN(24) DRN(20) DRN(16) DRN(12) DRN(8) DRN(4)
      "v_add_u32 v5, 1, v5\n\t" DRN(0)
      "s_endpgm"
      :: "s"(kb), "s"(vb), "v"(voff), "i"(NSLOT % RPU ? NSLOT % RPU : RPU), "i"(L_CT), "i"(SK * DH * 2), "i"(L_X), "i"(L_END), "i"(NSLOT), "i"(PACE), "i"(65536 / WQ), "i"((WIN - 12) / 2), "i"(NBLK), "i"(RPU),
         "v"(voff + 3072), "v"(voff + 2 * 3072), "v"(voff + 3 * 3072), "s"(tstart)
      : "memory", "scc", "v2", "v4", "v5", "v8", "v9", "v10", "v11", "s64", "s65", "s66", "s67", "s68", "s69", "s70", "s71", "s72", "s74", "s75", "s76", "s77", "s78", "s79", "s80", "s81",
        "s82", "s83", "s84", "s85", "s86", "s87", "s88");
#undef DRN
#undef VAR
#undef LD4
#undef LD1
}

__global__ void __launch_bounds__(NT, 3) fused_decode(Params p) {
  __shared__ __attribute__((aligned(16))) unsigned char smem[L_END];
  LAS unsigned char* L = (LAS unsigned char*)smem;
  const int tid = threadIdx.x, lane = tid & 63, w = __builtin_amdgcn_readfirstlane(tid >> 6);
  const int bid = blockIdx.x, xg = bid & 7, rk = bid >> 3;
  const int h = xg * 4 + (rk >> 3), b = (rk >> 2) & 1, sp = rk & 3;
  const int prod = b * 128 + h * 4 + sp;
  const int r = lane >> 4, c = lane & 15;
  const unsigned tstart = now32();
  u64 pc0;
  asm volatile("s_getpc_b64 %0" : "=s"(pc0));
  const __amdgpu_buffer_rsrc_t rsW = __builtin_amdgcn_make_buffer_rsrc((void*)p.ws, 0, (int)WS_END, 0x00020000);
  volatile LAS unsigned* ct = (volatile LAS unsigned*)(L + L_CT);
  ct[min(lane, 8)] = 0u;
  __syncthreads();
  __builtin_amdgcn_sched_barrier(0);
  unsigned e0, xa, xb, cpf;
  u32x4 x0, x1, wv[4];
  asm volatile("" : "=v"(e0), "=v"(xa), "=v"(xb), "=v"(cpf), "=v"(x0), "=v"(x1), "=v"(wv[0]), "=v"(wv[1]), "=v"(wv[2]), "=v"(wv[3]));
  if (__builtin_expect(w < NAW, 1)) {
    __builtin_amdgcn_s_setprio(3);
    const __amdgpu_buffer_rsrc_t rsQ = __builtin_amdgcn_make_buffer_rsrc((void*)p.wq, 0, DM * NH * DH * 2, 0x00020000);
    x0 = *(const GAS u32x4*)(p.x + lane * 8); x1 = *(const GAS u32x4*)(p.x + DM + lane * 8);
    const int qoff = ((256 * b + 64 * w + (lane & 60)) * (NH * DH) + h * DH + sp * 32 + (lane & 3) * 8) * 2;
#pragma unroll
    for (int i = 0; i < 4; ++i) wv[i] = __builtin_amdgcn_raw_buffer_load_b128(rsQ, qoff, i * (NH * DH * 2), 2);
    xa = p.x[256 * b + 64 * w + lane]; xb = p.x[DM + 256 * b + 64 * w + lane];
    e0 = __hip_atomic_load((GAS unsigned*)(p.ws + WS_E), RLX_AGENT);
    const __amdgpu_buffer_rsrc_t rsC = __builtin_amdgcn_make_buffer_rsrc((void*)(pc0 & ~255ull), 0, CODE_LINES * 128, 0x00020000);
    cpf = __builtin_amdgcn_raw_buffer_load_b32(rsC, lane < 2 ? ((rk * 4 + w) * 2 + lane) * 128 : OOB, 0, 0);
  }
  __builtin_amdgcn_sched_barrier(0);
  if (__builtin_expect(w == NAW + NSW, 1)) {
    const size_t kvbase = (size_t)(b * NH + h) * SK * DH;
    loader_wave(p.K + kvbase, p.V + kvbase, sp * 1024 + lane * 16, tstart);
  }
  int wr = w;
  asm volatile("" : "+s"(wr));
  if (__builtin_expect(wr < NAW, 1)) {
    const int rg = lane >> 2, c4 = lane & 3, gcol = (lane & 32) + 8 * c4 + 4 * (r & 1) + (rg & 3);
    const unsigned tag = e0 + 1u;
    float m0, d0, m1, d1;
    ln_stats(x0, m0, d0); ln_stats(x1, m1, d1);
    {
      LAS float* pq = (LAS float*)(L + L_PQ);
      pq[w * 64 + gcol] = gemv64x32(wv, ln_apply(bf_lo(xa), m0, d0), ln_apply(bf_lo(xb), m1, d1), lane);
      asm volatile("" :: "v"(tag));
      if (lds_arrive_last(L, 0)) {
        const int l2 = 2 * (lane & 31);
        const f32x2 s = (*(LAS f32x2*)(pq + l2) + *(LAS f32x2*)(pq + 64 + l2)) + (*(LAS f32x2*)(pq + 128 + l2) + *(LAS f32x2*)(pq + 192 + l2));
        st_gran2(rsW, lane < 32 ? (int)WS_QP + (((h * 4 + sp) * 2 + b) * 64 + l2) * 8 : OOB, tag, s[0], s[1]);
        const int qo = (int)WS_QP + (((h * 4 + r) * 2) * 64 + b * 32 + 2 * c) * 8;
        u32x4 g0, g1;
        __builtin_amdgcn_s_sleep(Q_WAIT);
        for (unsigned spins = 0; spins < SPIN_CAP; ++spins) {
          asm volatile("" ::: "memory");
          g0 = ld_gran2(rsW, qo); g1 = ld_gran2(rsW, qo + 512);
          if (__all(g0[1] == tag && g0[3] == tag && g1[1] == tag && g1[3] == tag)) break;
        }
        ((LAS unsigned*)(L + L_Q))[lane] = pkbf(__uint_as_float(g0[0]) + __uint_as_float(g1[0]), __uint_as_float(g0[2]) + __uint_as_float(g1[2]));
        if (lane == 0) { ct[3] = tag; __hip_atomic_store((LAS unsigned*)(L + L_CT) + 2, 1u, __ATOMIC_RELEASE, __HIP_MEMORY_SCOPE_WORKGROUP); }
      }
    }
    asm volatile("" :: "v"(cpf));
    __builtin_amdgcn_s_setprio(2);
    if (rk < 22 && (xg & 1) * 22 + rk < NSL) {
      const int j = (xg & 1) * 22 + rk, mat = (xg >> 1) & 1, hf = xg >> 2;
      wait_until(tstart, WO_T0 - 200);
      const unsigned ya = p.x[256 * hf + 64 * w + lane], yb = p.x[DM + 256 * hf + 64 * w + lane];
      const GAS u16* src = (const GAS u16*)(mat ? p.wu : p.wg) + (size_t)(256 * hf + 64 * w + 4 * rg) * FF + j * 32 + c4 * 8;
#pragma unroll
      for (int i = 0; i < 4; ++i) wv[i] = __builtin_nontemporal_load((const GAS u32x4*)(src + (size_t)i * FF));
      LAS float* pf = (LAS float*)(L + L_PF);
      pf[w * 64 + gcol] = gemv64x32(wv, ln_apply(bf_lo(ya), m0, d0), ln_apply(bf_lo(yb), m1, d1), lane);
      if (lds_arrive_last(L, 1)) {
        const int l2 = 2 * (lane & 31);
        const f32x2 s = (*(LAS f32x2*)(pf + l2) + *(LAS f32x2*)(pf + 64 + l2)) + (*(LAS f32x2*)(pf + 128 + l2) + *(LAS f32x2*)(pf + 192 + l2));
        st_gran2(rsW, lane < 32 ? (int)WS_GU + (((j * 2 + mat) * 2 + hf) * 64 + l2) * 8 : OOB, tag, s[0], s[1]);
      }
    }
    u32x4 bf[4][8];
    {
      const GAS u16* src = (const GAS u16*)p.wo + (size_t)(h * DH + 8 * r) * DM + w * 128 + c * 8;
#pragma unroll
      for (int s = 0; s < 4; ++s) {
        if (s == 1 && rk >= 22 && rk < 22 + NZG) {
          const int cg = rk - 22, u0 = 172 * xg + 43 * w, unit = u0 + lane;
          const bool live = lane < 43;
          const __amdgpu_buffer_rsrc_t rsD = __builtin_amdgcn_make_buffer_rsrc((void*)p.wd, 0, FF * DM * 2, 0x00020000);
          u32x4 dv[6];
#pragma unroll
          for (int i = 0; i < 6; ++i)
            dv[i] = __builtin_amdgcn_raw_buffer_load_b128(rsD, 8 * i + (lane >> 3) < 43 ? ((u0 + 8 * i + (lane >> 3)) * DM + 64 * cg + (lane & 7) * 8) * 2 : OOB, 0, 2);
          const int go = live ? (int)WS_GU + ((unit >> 5) * 256 + (unit & 31)) * 8 : OOB;
          u32x2 gv[8];
          for (unsigned spins = 0; spins < SPIN_CAP; ++spins) {
            asm volatile("" ::: "memory");
#pragma unroll
            for (int i = 0; i < 8; ++i) gv[i] = __builtin_amdgcn_raw_buffer_load_b64(rsW, go, i * 256, 16);
            bool ok = true;
#pragma unroll
            for (int i = 0; i < 8; ++i) ok &= gv[i][1] == tag;
            if (__all(ok || !live)) break;
            __builtin_amdgcn_s_sleep(32);
          }
          LAS float* hb = (LAS float*)(L + L_H) + w * 128;
#pragma unroll
          for (int bb = 0; bb < 2; ++bb) {
            const float gs = rbf(__uint_as_float(gv[bb][0]) + __uint_as_float(gv[2 + bb][0])), us = rbf(__uint_as_float(gv[4 + bb][0]) + __uint_as_float(gv[6 + bb][0]));
            hb[bb * 64 + lane] = live ? rbf(rbf(gelu_tanh(gs)) * us) : 0.f;
          }
          float z0[8], z1[8];
#pragma unroll
          for (int i = 0; i < 8; ++i) z0[i] = z1[i] = 0.f;
#pragma unroll
          for (int i = 0; i < 6; ++i) {
            const float a0 = hb[8 * i + (lane >> 3)], a1 = hb[64 + 8 * i + (lane >> 3)];
#pragma unroll
            for (int k = 0; k < 4; ++k) {
              const float lo = bf_lo(dv[i][k]), hi = bf_hi(dv[i][k]);
              z0[2 * k] = fmaf(a0, lo, z0[2 * k]); z0[2 * k + 1] = fmaf(a0, hi, z0[2 * k + 1]);
              z1[2 * k] = fmaf(a1, lo, z1[2 * k]); z1[2 * k + 1] = fmaf(a1, hi, z1[2 * k + 1]);
            }
          }
          float v[8], t[4];
#pragma unroll
          for (int i = 0; i < 8; ++i) v[i] = swap32_sum(z0[i], z1[i]);
#pragma unroll
          for (int i = 0; i < 4; ++i) { t[i] = swap16_sum(v[i], v[i + 4]); t[i] += dpp<ROR8>(t[i]); }
          const float t0 = t[0], t1 = t[1], t2 = t[2], t3 = t[3];
          LAS float* pf = (LAS float*)(L + L_PF);
          *(LAS f32x2*)(pf + w * 128 + (lane >> 5) * 64 + (lane & 7) * 8 + 4 * (r & 1) + ((lane & 8) >> 2)) = f32x2{(lane & 8) ? t2 : t0, (lane & 8) ? t3 : t1};
          if (lds_arrive_last(L, 1)) {
            const int l2 = 2 * lane;
            const f32x2 sm = (*(LAS f32x2*)(pf + l2) + *(LAS f32x2*)(pf + 128 + l2)) + (*(LAS f32x2*)(pf + 256 + l2) + *(LAS f32x2*)(pf + 384 + l2));
            st_gran2(rsW, (int)WS_Z + ((xg * 8 + cg) * 128 + l2) * 8, tag, sm[0], sm[1]);
          }
        }
        if (s == 3 && w == 2 && sp == 0) {
          const int zo = (int)WS_Z + ((r * 8 + (h >> 2)) * 128 + b * 64 + (h & 3) * 16 + c) * 8;
          const float xres = bf_lo(p.x[b * DM + h * 16 + c]);
          u32x2 zv[2];
          bool good = false;
          for (unsigned spins = 0; spins < SPIN_CAP; ++spins) {
            asm volatile("" ::: "memory");
            zv[0] = ld_gran(rsW, zo); zv[1] = ld_gran(rsW, zo + 4 * 8192);
            if (__all(zv[0][1] == tag && zv[1][1] == tag)) { good = true; break; }
            __builtin_amdgcn_s_sleep(16);
          }
          const float z = rbf(xrow_sum(__uint_as_float(zv[0][0]) + __uint_as_float(zv[1][0])));
          float g = rbf(z / (1.0f + __expf(-z)));
          if (!good) g = __uint_as_float(0x7fc00000u);
          if (lane < 16) { ((LAS float*)(L + L_G))[lane] = g; ((LAS float*)(L + L_G))[16 + lane] = xres; }
        }
        if (s == 3) {
          const unsigned c2 = __builtin_amdgcn_raw_buffer_load_b32(__builtin_amdgcn_make_buffer_rsrc((void*)(pc0 & ~255ull), 0, CODE_LINES * 128, 0x00020000),
                                                                   lane < 2 ? ((rk * 4 + w) * 2 + lane) * 128 + 64 : OOB, 0, 0);
          asm volatile("" :: "v"(c2));
        }
        u32x4 t[8];
#pragma unroll
        for (int j = 0; j < 8; ++j) {
          wait_until(tstart, WO_T0 + (8 * s + j) * WO_DT + (((h * 4 + w) * WO_DT) >> 7));
          __builtin_amdgcn_sched_barrier(0);
          t[j] = __builtin_nontemporal_load((const GAS u32x4*)(src + (32 * s + j) * DM));
          __builtin_amdgcn_sched_barrier(0);
        }
#pragma unroll
        for (int e = 0; e < 8; ++e)
#pragma unroll
          for (int k = 0; k < 4; ++k)
            bf[s][e][k] = __builtin_amdgcn_perm(t[2 * k + 1][e >> 1], t[2 * k][e >> 1], (e & 1) ? 0x07060302u : 0x05040100u);
#pragma unroll
        for (int e = 0; e < 8; ++e) asm volatile("" : "+v"(bf[s][e]));
        __builtin_amdgcn_sched_barrier(0);
      }
    }
    int yoff = (int)WS_Y + (prod * DM + w * 128 + c * 8 + 2 * r) * 8;
    unsigned ra = L_REC + (c & 7) * REC_STRIDE + 16 * r, rm = L_MLW + (r & 1) * 16;
    asm volatile("" : "+v"(yoff), "+v"(ra), "+v"(rm));
    __syncthreads();
    {
      u32x4 af[4];
      af[0] = *(LAS u32x4*)(L + ra);
      const f32x4 mlr = *(LAS f32x4*)(L + rm), mlo = *(LAS f32x4*)(L + (rm ^ 16));
#pragma unroll
      for (int s = 1; s < 4; ++s) af[s] = *(LAS u32x4*)(L + ra + 64 * s);
      __builtin_amdgcn_sched_barrier(0);
      f32x4 d[8];
#pragma unroll
      for (int e = 0; e < 8; ++e) d[e] = mfma16(af[0], bf[0][e], f32x4{0.f, 0.f, 0.f, 0.f});
      __builtin_amdgcn_sched_barrier(0);
      const float M = fmaxf(fmaxf(mlr[0], mlr[2]), fmaxf(mlo[0], mlo[2]));
      float fa = __builtin_amdgcn_exp2f(mlr[0] - M), fb = __builtin_amdgcn_exp2f(mlr[2] - M);
      const float Lh = fmaf(fa, mlr[1], fb * mlr[3]);
      const float Ls = swap16_sum(Lh, Lh);
      const float inv = __builtin_amdgcn_rcpf(Ls);
      fa *= inv; fb *= inv;
      const unsigned lse = __float_as_uint(M + __builtin_amdgcn_logf(Ls));
      unsigned w1 = (tag << 16) | (lse >> 16), w3 = (((tag << 16) ^ tag) & 0xffff0000u) | (lse & 0xffffu);
#pragma unroll
      for (int s = 1; s < 3; ++s)
#pragma unroll
        for (int e = 0; e < 8; ++e) d[e] = mfma16(af[s], bf[s][e], d[e]);
#pragma unroll
      for (int i = 0; i < 16; ++i) { __builtin_amdgcn_sched_group_barrier(0x8, 1, 0); __builtin_amdgcn_sched_group_barrier(0x402, 2, 0); }
      __builtin_amdgcn_sched_barrier(0);
      float u[8];
#pragma unroll
      for (int e = 0; e < 8; ++e) {
        d[e] = mfma16(af[3], bf[3][e], d[e]);
        u[e] = fmaf(fa, d[e][0] + d[e][1], fb * (d[e][2] + d[e][3]));
      }
      __builtin_amdgcn_sched_group_barrier(0x8, 3, 1);
#pragma unroll
      for (int i = 0; i < 5; ++i) { __builtin_amdgcn_sched_group_barrier(0x2, 3, 1); __builtin_amdgcn_sched_group_barrier(0x8, 1, 1); }
      float t[4];
#pragma unroll
      for (int i = 0; i < 4; ++i) { const float lo = u[i], hi = u[4 + i]; t[i] = (r & 2) ? hi : lo; }
      const float y0 = swap16_sum(t[0], t[2]), y1 = swap16_sum(t[1], t[3]);
      __builtin_amdgcn_raw_buffer_store_b128(u32x4{__float_as_uint(y0), w1, __float_as_uint(y1), w3}, rsW, yoff, 0, 16);
    }
    return;
  }
  if (w == NAW + NSW + 1) {
    while (__hip_atomic_load((LAS unsigned*)(L + L_CT) + 2, __ATOMIC_ACQUIRE, __HIP_MEMORY_SCOPE_WORKGROUP) == 0u) __builtin_amdgcn_s_sleep(8);
    const unsigned tag = ct[3];
#pragma unroll 1
    for (int k = 0; k < 2 * 32; ++k) {
      const unsigned due = SYNC_T0 + (k >> 1) * SYNC_T + (k & 1) * SYNC_D;
      int landed;
      while ((landed = (int)ct[8]) < 2 * NBLK - SYNC_STOP && now32() - tstart < due) __builtin_amdgcn_s_sleep(4);
      if (landed >= 2 * NBLK - SYNC_STOP) break;
      const int lo = (int)WS_PT + (k >> 1) * 4352;
      if ((k & 1) == 0) {
        if ((rk & 15) == 5 && lane == 0) __builtin_amdgcn_raw_buffer_store_b64(u32x2{now32() - ((landed * PACE) >> 8), tag}, rsW, lo + (xg + (rk & 16 ? 8 : 0)) * 8, 0, 16);
      } else {
        const u32x2 g = ld_gran(rsW, lo + c * 8);
        const int dlt = (int)row_sum((float)(int)(g[0] - tstart)) >> 4;
        if (__all(g[1] == tag) && lane == 0) ct[9] = tstart + (unsigned)(dlt - SYNC_OFF);
      }
    }
    return;
  }
  const int ws = w - NAW;
  while (__hip_atomic_load((LAS unsigned*)(L + L_CT) + 2, __ATOMIC_ACQUIRE, __HIP_MEMORY_SCOPE_WORKGROUP) == 0u) __builtin_amdgcn_s_sleep(1);
  const u32x4 qv = *(LAS u32x4*)(L + L_Q + c * 16);
  const unsigned tag = ct[3];
  float m = -INFINITY, l = 0.f;
  f32x2 acc[4];
#pragma unroll
  for (int i = 0; i < 4; ++i) acc[i] = f32x2{0.f, 0.f};
  constexpr float SCALE = 0.08838834764831845f, LOG2E = 1.4426950408889634f;
  __builtin_amdgcn_s_setprio(1);
  int slot = ws;
#pragma unroll 1
  for (int j = 0; j < NSTEP; ++j) {
    const int i = 4 * j + ws;
    const LAS unsigned char* xp = L + L_X + slot * 8192 + lane * 16;
    slot = slot + 4 >= NSLOT ? slot + 4 - NSLOT : slot + 4;
    u32x4 kk[4], vv[4];
    int landed;
    while ((landed = (int)__builtin_amdgcn_readfirstlane(ct[8])) < 2 * i + 1) __builtin_amdgcn_s_sleep(1);
    asm volatile("" ::: "memory");
#pragma unroll
    for (int u = 0; u < 4; ++u) kk[u] = *(const LAS u32x4*)(xp + u * 1024);
    float sc[4];
#pragma unroll
    for (int u = 0; u < 4; ++u) {
      float t = dot2(kk[u][0], qv[0], 0.f);
      t = dot2(kk[u][1], qv[1], t); t = dot2(kk[u][2], qv[2], t); t = dot2(kk[u][3], qv[3], t);
      sc[u] = rbf(rbf(row_sum(t)) * SCALE) * LOG2E;
    }
    const float mx = fmaxf(m, xrow_max(fmaxf(fmaxf(sc[0], sc[1]), fmaxf(sc[2], sc[3]))));
    const float al = __builtin_amdgcn_exp2f(m - mx);
    m = mx;
    float pp[4];
#pragma unroll
    for (int u = 0; u < 4; ++u) pp[u] = __builtin_amdgcn_exp2f(sc[u] - mx);
    l = fmaf(l, al, (pp[0] + pp[1]) + (pp[2] + pp[3]));
#pragma unroll
    for (int k = 0; k < 4; ++k) acc[k] *= al;
#pragma unroll
    for (int k = 0; k < 4; ++k) asm volatile("" : "+v"(acc[k]), "+v"(pp[k]));
    if (j == NSTEP - 1) {
      const float lw = xrow_sum(l);
      if (lane == 0) *(LAS f32x2*)(L + L_MLW + ws * 8) = f32x2{m, lw};
    }
    while (landed < 2 * (i + 1) + 2) {
      if (j != NSTEP - 1) __builtin_amdgcn_s_sleep(1);
      landed = (int)__builtin_amdgcn_readfirstlane(ct[8]);
    }
    asm volatile("" ::: "memory");
#pragma unroll
    for (int u = 0; u < 4; ++u) vv[u] = *(const LAS u32x4*)(xp + 4096 + u * 1024);
    asm volatile("" ::: "memory");
    ct[4 + ws] = (unsigned)(j + 1);
#pragma unroll
    for (int u = 0; u < 4; ++u)
#pragma unroll
      for (int k = 0; k < 4; ++k)
        acc[k] = __builtin_elementwise_fma(f32x2{pp[u], pp[u]}, f32x2{bf_lo(vv[u][k]), bf_hi(vv[u][k])}, acc[k]);
  }
  {
    float t[4];
#pragma unroll
    for (int i = 0; i < 4; ++i) t[i] = swap32_sum(acc[i >> 1][i & 1], acc[2 + (i >> 1)][i & 1]);
    const float c0 = swap16_sum(t[0], t[2]), c1 = swap16_sum(t[1], t[3]);
    const unsigned hi = pkbf(c0, c1);
    const unsigned lo = pkbf(c0 - bf_lo(hi), c1 - bf_hi(hi));
    *(LAS unsigned*)(L + L_REC + (2 * ws) * REC_STRIDE + (8 * c + 2 * r) * 2) = hi;
    *(LAS unsigned*)(L + L_REC + (2 * ws + 1) * REC_STRIDE + (8 * c + 2 * r) * 2) = lo;
  }
  __syncthreads();
  if (sp != 0) return;
  {
    int yo = (int)WS_Y + ((b * 128 + (8 * ws + (lane >> 5)) * 4 + (lane & 3)) * DM + 16 * h + 2 * ((lane >> 2) & 7)) * 8;
    unsigned ta = tag << 16, tb = (tag << 16) ^ tag;
    unsigned wa = L_RED + (2 * ((lane >> 2) & 7) + (lane >> 5)) * 16 + ws * 4, oo = (b * DM + h * 16 + c) * 2;
    float g = ((LAS float*)(L + L_G))[c], xres = ((LAS float*)(L + L_G))[16 + c];
    asm volatile("" : "+v"(yo), "+v"(ta), "+v"(tb), "+v"(wa), "+v"(oo), "+v"(g), "+v"(xres));
    {
      const unsigned t0 = now32();
      const int due = min(max(C_MIN, (int)((unsigned)__builtin_amdgcn_readfirstlane(ct[9]) + PACE + C_OFF - t0)), C_MAX);
      while ((int)(now32() - t0) < due) __builtin_amdgcn_s_sleep(1);
    }
    u32x4 yv[4];
#pragma unroll
    for (int i = 0; i < 4; ++i) yv[i] = __builtin_amdgcn_raw_buffer_load_b128(rsW, yo, i * (8 * DM * 8), 16);
    float t;
    for (unsigned spins = 0;; ++spins) {
      unsigned bad[4];
      float s0 = 0.f, s1 = 0.f;
#pragma unroll
      for (int i = 0; i < 4; ++i) {
        bad[i] = (yv[i][1] ^ ta) | (yv[i][3] ^ tb);
        const float lse = __uint_as_float(__builtin_amdgcn_perm(yv[i][1], yv[i][3], 0x05040100u));
        const float M = quad_max(lse);
        const float e = __builtin_amdgcn_exp2f(lse - M);
        float S = e + dpp<XOR1>(e); S += dpp<XOR2>(S);
        const float wt = e * __builtin_amdgcn_rcpf(S);
        s0 = fmaf(wt, __uint_as_float(yv[i][0]), s0); s1 = fmaf(wt, __uint_as_float(yv[i][2]), s1);
      }
      t = swap32_sum(s0, s1);
      t += dpp<XOR1>(t); t += dpp<XOR2>(t);
      asm volatile("" : "+v"(t));
      if (__builtin_expect(__builtin_amdgcn_ballot_w64(((bad[0] | bad[1]) | (bad[2] | bad[3])) > 0xffffu) == 0, 1)) break;
      if (spins >= SPIN_CAP) { t = __uint_as_float(0x7fc00000u); break; }
      __builtin_amdgcn_s_sleep(1);
      u32x4 ty[4];
      bool again[4];
      asm volatile("" ::: "memory");
#pragma unroll
      for (int i = 0; i < 4; ++i) {
        again[i] = __builtin_amdgcn_ballot_w64(bad[i] > 0xffffu) != 0;
        ty[i] = __builtin_amdgcn_raw_buffer_load_b128(rsW, yo, again[i] ? i * (8 * DM * 8) : 0x40000000, 16);
      }
      asm volatile("" ::: "memory");
#pragma unroll
      for (int i = 0; i < 4; ++i) {
        const u32x4 o = yv[i], n = ty[i];
        yv[i] = again[i] ? n : o;
      }
    }
    *(LAS float*)(L + wa) = t;
    __syncthreads();
    if (ws == 0) {
      const f32x4 rv = *(LAS f32x4*)(L + L_RED + c * 16);
      const float sv = rbf((rv[0] + rv[1]) + (rv[2] + rv[3]));
      if (lane < 16) *(GAS u16*)((GAS unsigned char*)p.out + oo) = (u16)bfbits(xres + rbf(sv * g));
      if (prod == 0 && lane == 0) __hip_atomic_store((GAS unsigned*)(p.ws + WS_E), tag, RLX_AGENT);
    }
  }
}

extern "C" void kernel_launch(void* const* d_in, const int* in_sizes, int n_in, void* d_out, int out_size, void* d_ws, size_t ws_size,
                              hipStream_t stream) {
  Params p;
  p.x = (const u16*)d_in[0]; p.K = (const u16*)d_in[1]; p.V = (const u16*)d_in[2]; p.wq = (const u16*)d_in[3]; p.wo = (const u16*)d_in[4];
  p.wg = (const u16*)d_in[6]; p.wu = (const u16*)d_in[7]; p.wd = (const u16*)d_in[8];
  p.out = (u16*)d_out; p.ws = (unsigned char*)d_ws;
  fused_decode<<<NWG, NT, 0, stream>>>(p);
}
```
